```python
import torch

def _softmax(x, axis=-1):
    return torch.softmax(x, dim=axis)

def _gelu(x):
    return torch.nn.functional.gelu(x, approximate='tanh')

def _silu(x):
    return torch.nn.functional.silu(x)

def _sigmoid(x):
    return torch.sigmoid(x)

def _layernorm(x, axis=-1, eps=1e-05):
    m = x.mean(dim=axis, keepdim=True)
    v = x.var(dim=axis, keepdim=True, unbiased=False)
    return (x - m) / torch.sqrt(v + eps)

def _scatter_add(tgt, idx, src, axis=0):
    return tgt.clone().index_add_(axis, idx, src)

def _segment_sum(src, seg, n):
    out = torch.zeros((n,) + src.shape[1:], dtype=src.dtype)
    return out.index_add_(0, seg, src)

def _pad(x, pw):
    flat = tuple((v for pair in reversed(pw) for v in pair))
    return torch.nn.functional.pad(x, flat)
kernel_name = 'parallel_sum__memory_aug_recurrent_mem_tr__mixer_token_mlp__prompt'

def setup_inputs():

    _w = lambda a: (lambda t: t.to(torch.bfloat16) if t.is_floating_point() else t)(torch.as_tensor(a))

    def _A_setup():
        B, S, Dm = (1, 2048, 576)
        H, Dh = (32, 128)
        M, seg = (8, 32)
        seg = min(seg, S)
        S = S // seg * seg
        return {'x': _w((lambda *s: torch.randn(*s, dtype=torch.bfloat16) * 0.05)(B, S, Dm)), 'mem0': _w((lambda *s: torch.randn(*s, dtype=torch.bfloat16) * 0.2)(B, M, Dm)), 'Wq': _w((lambda *s: torch.randn(*s, dtype=torch.bfloat16) * 1.6)(Dm, H, Dh) * Dm ** (-0.5)), 'Wk': _w((lambda *s: torch.randn(*s, dtype=torch.bfloat16) * 1.6)(Dm, H, Dh) * Dm ** (-0.5)), 'Wv': _w((lambda *s: torch.randn(*s, dtype=torch.bfloat16) * 0.2)(Dm, H, Dh) * Dm ** (-0.5)), 'Wo': _w((lambda *s: torch.randn(*s, dtype=torch.bfloat16) * 0.2)(H, Dh, Dm) * (H * Dh) ** (-0.5)), 'seg_len': seg}
    _A_in = _A_setup()

    def _B_setup():
        B, S, D, F = (1, 2048, 576, 4608)
        return {'x': _w((lambda *s: torch.randn(*s, dtype=torch.bfloat16) * 0.05)(B, S, D)), 'w1': _w((lambda *s: torch.randn(*s, dtype=torch.bfloat16) * 0.4)(S, F) * S ** (-0.5)), 'b1': _w((lambda *s: torch.randn(*s, dtype=torch.bfloat16) * 3.2)(F) * 0.02), 'w2': _w((lambda *s: torch.randn(*s, dtype=torch.bfloat16) * 0.2)(F, S) * F ** (-0.5)), 'b2': _w((lambda *s: torch.randn(*s, dtype=torch.bfloat16) * 0.4)(S) * 0.02)}
    _B_in = _B_setup()
    return {'A_x': _A_in['x'], 'A_mem0': _A_in['mem0'], 'A_Wq': _A_in['Wq'], 'A_Wk': _A_in['Wk'], 'A_Wv': _A_in['Wv'], 'A_Wo': _A_in['Wo'], 'A_seg_len': _A_in['seg_len'], 'B_x': _B_in['x'], 'B_w1': _B_in['w1'], 'B_b1': _B_in['b1'], 'B_w2': _B_in['w2'], 'B_b2': _B_in['b2']}

def reference(A_x, A_mem0, A_Wq, A_Wk, A_Wv, A_Wo, A_seg_len, B_x, B_w1, B_b1, B_w2, B_b2):

    def _A_ref(x, mem0, Wq, Wk, Wv, Wo, seg_len):
        B, S, Dm = x.shape
        H, Dh = (Wq.shape[1], Wq.shape[2])
        M = mem0.shape[1]
        scale = Dh ** (-0.5)
        n_seg = S // seg_len
        L = M + seg_len
        idx = torch.arange(L)
        mask = (idx[:, None] < M) | (idx[:, None] >= idx[None, :])
        mem = mem0
        outs = []
        for s in range(n_seg):
            xs = x[:, s * seg_len:(s + 1) * seg_len, :].clone()
            h = torch.concatenate([mem, xs], axis=1)
            q = torch.einsum('btd,dhe->bhte', h, Wq)
            k = torch.einsum('btd,dhe->bhte', h, Wk)
            v = torch.einsum('btd,dhe->bhte', h, Wv)
            scores = torch.einsum('bhqe,bhke->bhqk', q, k) * scale
            scores = torch.where(mask[None, None], scores, -1000000000.0)
            p = _softmax(scores, axis=-1)
            o = torch.einsum('bhqk,bhke->bhqe', p, v)
            o = torch.einsum('bhte,hed->btd', o, Wo)
            mem = o[:, :M, :].clone()
            outs.append(o[:, M:, :])
        return torch.concatenate(outs, axis=1)

    def _B_ref(x, w1, b1, w2, b2):
        B, S, D = x.shape
        h = _layernorm(x, axis=-1)
        u = torch.einsum('bsd,sf->bdf', h, w1) + b1
        u = _gelu(u)
        y = torch.einsum('bdf,fs->bds', u, w2) + b2
        return (lambda x, *d: x.permute(*d))(y, 0, 2, 1)
    x = A_x
    xn = _layernorm(x, axis=-1)
    return x + _A_ref(xn, A_mem0, A_Wq, A_Wk, A_Wv, A_Wo, A_seg_len) + _B_ref(xn, B_w1, B_b1, B_w2, B_b2)
```

```cpp
#include <hip/hip_runtime.h>
#include <hip/hip_bf16.h>

#define CLV4 "v4","v5","v6","v7","v8","v9","v10","v11","v12","v13","v14","v15","v16","v17","v18","v19","v20","v21","v22","v23","v24","v25","v26","v27","v28","v29","v30","v31","v32","v33","v34","v35","v36","v37","v38","v39","v40","v41","v42","v43","v44","v45","v46","v47","v48","v49","v50","v51","v52","v53","v54","v55","v56","v57","v58","v59","v60","v61","v62","v63","v64","v65","v66","v67","v68","v69","v70","v71","v72","v73","v74","v75","v76","v77","v78","v79","v80","v81","v82","v83","v84","v85","v86","v87","v88","v89","v90","v91","v92","v93","v94","v95","v96","v97","v98","v99","v100","v101","v102","v103","v104","v105","v106","v107","v108","v109","v110","v111","v112","v113","v114","v115","v116","v117","v118","v119","v120","v121","v122","v123","v124","v125","v126","v127","v128","v129","v130","v131","v132","v133","v134","v135","v136","v137","v138","v139","v140","v141","v142","v143","v144","v145","v146","v147","v148","v149","v150","v151","v152","v153","v154","v155","v156","v157","v158","v159","v160","v161","v162","v163","v164","v165","v166","v167","v168","v169","v170","v171","v172","v173","v174","v175","v176","v177","v178","v179","v180","v181","v182","v183","v184","v185","v186","v187","v188","v189","v190","v191","v192","v193","v194","v195","v196","v197","v198","v199","v200","v201","v202","v203","v204","v205","v206","v207","v208","v209","v210","v211","v212","v213","v214","v215","v216","v217","v218","v219","v220","v221","v222","v223","v224","v225","v226","v227","v228","v229","v230","v231","v232","v233","v234","v235"
#define CLS "s36","s37","s38","s39","s40","s41","s42","s43","s44","s45","s46","s47","s48","s49","s50","s51","s52","s53","s54","s55","s56","s57","s58","s59","s60","s61","s62","s63"
namespace {
typedef unsigned short u16;
typedef unsigned v4u __attribute__((ext_vector_type(4)));
typedef unsigned v2u __attribute__((ext_vector_type(2)));
typedef float v4f __attribute__((ext_vector_type(4)));
typedef short v4s __attribute__((ext_vector_type(4)));
typedef __bf16 v8bf __attribute__((ext_vector_type(8)));
typedef __bf16 v2bf __attribute__((ext_vector_type(2)));
typedef float v2f __attribute__((ext_vector_type(2)));
#define LDSP(T, off) ((__attribute__((address_space(3))) T*)(size_t)(off))
constexpr unsigned H2BASE = 131072u;
constexpr unsigned H1BASE = 524288u;
constexpr unsigned ALM = 65536u;
constexpr unsigned TSB = 49152u;
constexpr unsigned LNF = 0u;
constexpr unsigned KVP = 69632u;
constexpr unsigned CKP = 81920u;
constexpr unsigned G1F = 8192u;
constexpr unsigned G2F = 20480u;
constexpr unsigned QOP = 32768u;
constexpr unsigned FLAG_BYTES = 114688u;
constexpr unsigned REQ = 327680u;
constexpr unsigned RSP = 360448u;
constexpr unsigned DUM = 262144u;
constexpr unsigned PARW = 40960u;
constexpr unsigned XNF = 2097152u;
constexpr unsigned H2F = 4718592u;
constexpr unsigned UF = 7340032u;
constexpr unsigned KMF = 134217728u;
constexpr unsigned VMF = 134221824u;
constexpr unsigned KF = 67108864u;
constexpr unsigned VF = 67633152u;
constexpr unsigned PART = 67108864u;
constexpr unsigned W2C = 16777216u;
constexpr unsigned YB = 13631488u;
constexpr unsigned WSREC = 226492416u;
constexpr unsigned OOB = 1879048192u;
constexpr unsigned TB = 131072u;
constexpr unsigned TBG = 147456u;
constexpr unsigned TBC = 114688u;
constexpr unsigned LQK = 9216u;
constexpr unsigned LKX = 16384u;
constexpr unsigned LRES = 49152u;
constexpr unsigned RES = 26624u;
constexpr unsigned LWO9 = 155648u;
constexpr unsigned LBL_C0 = 0u;
constexpr unsigned LBL_C1 = 2u;
constexpr unsigned HSPLIT = 0u;
constexpr unsigned XP = 0u;
constexpr unsigned CPFIRST = 0u;
constexpr unsigned CPB = 4u;
constexpr unsigned RPACE = 6u;
constexpr unsigned CODEPF = 1u;
constexpr unsigned TOUCH = 0u;
constexpr unsigned KGATE = 0u;
constexpr unsigned FLIP14 = 16384u;
constexpr unsigned AFAR = 0u;
constexpr unsigned XFAR = 0u;
constexpr unsigned RINGS = 163840 + 20 * 32768;
constexpr int LDS_BYTES = 163840;
constexpr unsigned SPINS = 1u << 19;

struct Params {
  const u16 *x, *mem0, *wq, *wk, *wv, *wo, *w1, *b1, *w2, *b2;
  u16* out; char* ws;
};
__device__ __forceinline__ unsigned lo32(const void* p) { return (unsigned)(size_t)p; }
__device__ __forceinline__ unsigned hi32(const void* p) { return (unsigned)((size_t)p >> 32); }
__device__ __forceinline__ unsigned pk2(float a, float b) { v2f f = {a, b}; return __builtin_bit_cast(unsigned, __builtin_convertvector(f, v2bf)); }
__device__ __forceinline__ float blo(unsigned v) { return __uint_as_float(v << 16); }
__device__ __forceinline__ float bhi(unsigned v) { return __uint_as_float(v & 0xffff0000u); }
__device__ __forceinline__ unsigned taskoff(int t) {
  const int j = t < 64 ? (t >> 5) * 16 + (t & 15) : t - 32, far = t < 64 ? ((t >> 4) & 1) ^ AFAR : XFAR;
  const unsigned a = H1BASE + (j >> 1) * 32768 + (j & 1) * 4096;
  return a | ((a >> 19 & 1) ^ far) << 14;
}
__device__ __forceinline__ int fswz(int lp) { return (0x78 >> (2 * lp)) & 3; }
__device__ __forceinline__ int wait_ge(__amdgpu_buffer_rsrc_t rs, unsigned off, int n, int need, int lane, unsigned& budget, bool quick = false) {
  const unsigned o = off + (lane < n ? lane : 0) * 128;
  int v;
  for (;;) {
    asm volatile("" ::: "memory");
    v = lane < n ? (int)__builtin_amdgcn_raw_buffer_load_b32(rs, o, 0, 16) : 0x7fffffff;
    if (__all(v >= need) || ++budget >= SPINS) break;
    if (quick) __builtin_amdgcn_s_sleep(1); else __builtin_amdgcn_s_sleep(8);
  }
  asm volatile("" ::: "memory");
  return v;
}
__device__ __forceinline__ void publish(__amdgpu_buffer_rsrc_t rs, unsigned off, int tid, unsigned v = 0) {
  asm volatile("s_waitcnt vmcnt(0)" ::: "memory");
  __syncthreads();
  if (tid == 0) __builtin_amdgcn_raw_buffer_store_b32(v, rs, off, 0, 16);
}

__device__ __forceinline__ unsigned greet_post(__amdgpu_buffer_rsrc_t ws, unsigned found, int lane) {
  unsigned c = __builtin_amdgcn_readlane(found, 0) + 1;
  c += c == __builtin_amdgcn_readlane(found, 1);
  __builtin_amdgcn_raw_buffer_store_b32(c, ws, lane == 0 ? REQ + blockIdx.x * 128 : OOB, 0, 16);
  return c;
}
__device__ __forceinline__ void greet_wait(__amdgpu_buffer_rsrc_t ws, unsigned c, int lane, unsigned& budget) {
  v2u r;
  for (;;) {
    asm volatile("" ::: "memory");
    r = __builtin_amdgcn_raw_buffer_load_b64(ws, lane == 0 ? RSP + blockIdx.x * 128 : OOB, 0, 16);
    if (__builtin_amdgcn_readfirstlane(r.x) == c || ++budget >= SPINS) break;
    __builtin_amdgcn_s_sleep(2);
  }
  __builtin_amdgcn_raw_buffer_store_b32(r.y, ws, lane == 0 ? REQ + blockIdx.x * 128 + 4 : OOB, 0, 16);
  asm volatile("" ::: "memory");
}
__device__ __forceinline__ void greet_host(__amdgpu_buffer_rsrc_t ws, int tid, unsigned& budget) {
  const v4u ones = {~0u, ~0u, ~0u, ~0u};
  v2u q = __builtin_amdgcn_raw_buffer_load_b64(ws, REQ + tid * 128, 0, 16);
#pragma unroll
  for (int i = 0; i < FLAG_BYTES / 4096; ++i) __builtin_amdgcn_raw_buffer_store_b128(ones, ws, tid * 16, i * 4096, 16);
  asm volatile("s_waitcnt vmcnt(0)" : "+v"(q) :: "memory");
  __syncthreads();
  const unsigned n = q.y + 1;
  unsigned last = q.x + 1;
  bool open = tid != blockIdx.x;
  for (;;) {
    asm volatile("" ::: "memory");
    q = __builtin_amdgcn_raw_buffer_load_b64(ws, open ? REQ + tid * 128 : OOB, 0, 16);
    open = open && q.y != n;
    const v2u r = {q.x, n};
    __builtin_amdgcn_raw_buffer_store_b64(r, ws, open && (q.x != last || budget == 0) ? RSP + tid * 128 : OOB, 0, 16);
    last = q.x;
    if (!__any(open) || ++budget >= SPINS) break;
    __builtin_amdgcn_s_sleep(4);
  }
}

__global__ void __launch_bounds__(256, 1) main_kernel(Params p) {
  int tid = threadIdx.x;
  const int l = tid & 63, w = tid >> 6, c = l & 15, g = l >> 4, q = (l >> 2) & 3, pp = l & 3;
  const int label = blockIdx.x & 7, rank = blockIdx.x >> 3;
  const __amdgpu_buffer_rsrc_t ws = __builtin_amdgcn_make_buffer_rsrc(p.ws, 0, WSREC, 0x00020000);
  const unsigned l16 = l * 16, g1 = w * 1024 + l16, g3 = w * 256 + l * 4;
  if (CODEPF && rank < 2) {
    asm volatile("s_getpc_b64 s[40:41]\n .Lpf_pc:\n s_mov_b32 s42, .Lcode_end-.Lpf_pc\n s_mov_b32 s43, 0x20000\n s_mov_b32 m0, 98304\n s_nop 0\n"
                 "buffer_load_dword %0, s[40:43], 0 offen lds\n"
                 :: "v"(tid * 128 + rank * 32768) : "s40", "s41", "s42", "s43", "memory");
  }
  const unsigned hsa = l < 2 ? (l ? RSP : REQ) + blockIdx.x * 128 : OOB;
  unsigned budget = 0;
#define TAB(base, k) (*LDSP(unsigned, (base) + (k) * 1024 + tid * 4))
  if (label == LBL_C0 || label == LBL_C1) {
    const int h = HSPLIT ? (rank >> 1) + (label == LBL_C1 ? 16 : 0) : rank, grp = HSPLIT ? rank & 1 : label == LBL_C1;
    TAB(TBC, 0) = (16 * w + g) * 8192 + h * 256 + (((c >> 1) ^ ((g >> 1) | (w << 1))) * 2 + (c & 1)) * 16;
    const int kr = (g & 1) | (q << 1);
    TAB(TBC, 2) = (g >> 1) * 4096 + q * 1024 + (g & 1) * 512 + ((2 * w) ^ kr) * 32 + pp * 8;
    TAB(TBC, 3) = (g >> 1) * 4096 + q * 1024 + (g & 1) * 512 + ((2 * w + 1) ^ kr) * 32 + pp * 8;
    TAB(TBC, 4) = (TOUCH && w == 0 && (l >> 1) == 1) ? KF + (2 * rank + (l & 1)) * (2u << 20) : hsa;
    TAB(TBC, 5) = (l >> 3) * 1152 + (grp ? 640 : 0) + ((l >> 2) & 1) * 256 + w * 64 + pp * 16;
    TAB(TBC, 6) = (32 * w + (l >> 2)) * 1152 + (256 + 32 * grp) * 2 + pp * 16;
    TAB(TBC, 7) = (g & 1) * 8192 + (4 * (g >> 1) + (q & 1)) * 128 + (q >> 1) * 32 + pp * 8 + w * 1024;
    TAB(TBC, 8) = (g & 1) * 4096 + (4 * (g >> 1) + (q & 1)) * 64 + (q >> 1) * 32 + pp * 8 + w * 1024;
    TAB(TBC, 9) = LWO9 + w * 2048 + l16;
    const int task = w == 0 ? 32 * grp + h : (w == 1 && (h & 19) == 0 ? 64 + 2 * (h >> 2) + grp : (w == 2 ? 73 : 72));
    TAB(TBC, 1) = taskoff(task);
    TAB(TBC, 10) = (c & 2) ? 0xEEEEu : 0x4444u;
    TAB(TBC, 11) = l16;
    TAB(TBC, 12) = LRES + w * RES + l16;
    TAB(TBC, 13) = g1;
    TAB(TBC, 14) = task == 72 ? OOB : (l >= 32 ? 16384 + (l - 32) * 64 : (l < 16 && (w == 2 || HSPLIT) ? l * 512 : (l == 16 && w == 2 ? KVP - TSB + h * 128 : OOB)));
    TAB(TBC, 15) = LQK + g * 1088 + (32 * w + c) * 8;
    TAB(TBC, 16) = LQK + pp * 1088 + (4 * g + q) * 8;
    TAB(TBC, 17) = (l & ~2) * 16;
    TAB(TBC, 18) = (taskoff(32 * grp + 8 * (w >> 1) + 2 * g + (w & 1)) & ~16384u) + h * 128 + c * 8;
    TAB(TBC, 19) = w == 3 ? taskoff(64 + 2 * g + grp) + h * 128 + c * 8 : OOB;
    TAB(TBC, 20) = (task < 72 && c < 8 && g < 2) ? (task & 63) * 128 + g * 64 + ((c >> 1) ^ fswz((task & 7) >> 1)) * 16 + (c & 1) * 8 : OOB;
    TAB(TBC, 21) = c * 64 + (g ^ fswz(c >> 2)) * 16;
    TAB(TBC, 22) = l == 0 ? h * 128 : OOB;
    TAB(TBC, 23) = LKX + l16;
    TAB(TBC, 28) = lo32(p.ws); TAB(TBC, 29) = hi32(p.ws);
    TAB(TBC, 30) = lo32(p.wq); TAB(TBC, 31) = hi32(p.wq); TAB(TBC, 32) = lo32(p.wk); TAB(TBC, 33) = hi32(p.wk);
    TAB(TBC, 34) = lo32(p.wv); TAB(TBC, 35) = hi32(p.wv);
    TAB(TBC, 36) = lo32(p.wo + h * 128 * 576); TAB(TBC, 37) = hi32(p.wo + h * 128 * 576);
    TAB(TBC, 38) = w * 1024; TAB(TBC, 39) = h | grp << 8 | (rank + (label == LBL_C1 ? 32 : 0)) << 10 | task << 16;
    asm volatile("s_waitcnt lgkmcnt(0)\n"
      "v_lshlrev_b32 v15, 2, v0\n"
      "v_add_u32 v15, 114688, v15\n"
      "ds_read_b32 v1, v15 offset:0\n"
      "ds_read_b32 v2, v15 offset:1024\n"
      "ds_read_b32 v3, v15 offset:2048\n"
      "ds_read_b32 v4, v15 offset:3072\n"
      "ds_read_b32 v5, v15 offset:4096\n"
      "ds_read_b32 v9, v15 offset:5120\n"
      "ds_read_b32 v7, v15 offset:6144\n"
      "ds_read_b32 v8, v15 offset:7168\n"
      "ds_read_b32 v6, v15 offset:8192\n"
      "ds_read_b32 v10, v15 offset:9216\n"
      "s_waitcnt lgkmcnt(0)\n"
      "v_readfirstlane_b32 s67, v2\n"
      "ds_read_b32 v16, v15 offset:28672\n"
      "ds_read_b32 v17, v15 offset:29696\n"
      "ds_read_b32 v18, v15 offset:30720\n"
      "ds_read_b32 v19, v15 offset:31744\n"
      "ds_read_b32 v20, v15 offset:32768\n"
      "ds_read_b32 v21, v15 offset:33792\n"
      "ds_read_b32 v22, v15 offset:34816\n"
      "ds_read_b32 v23, v15 offset:35840\n"
      "ds_read_b32 v24, v15 offset:36864\n"
      "ds_read_b32 v25, v15 offset:37888\n"
      "ds_read_b32 v26, v15 offset:38912\n"
      "ds_read_b32 v27, v15 offset:39936\n"
      "s_waitcnt lgkmcnt(0)\n"
      "v_readfirstlane_b32 s40, v16\n"
      "v_readfirstlane_b32 s41, v17\n"
      "v_readfirstlane_b32 s84, v18\n"
      "v_readfirstlane_b32 s85, v19\n"
      "v_readfirstlane_b32 s86, v20\n"
      "v_readfirstlane_b32 s87, v21\n"
      "v_readfirstlane_b32 s88, v22\n"
      "v_readfirstlane_b32 s89, v23\n"
      "v_readfirstlane_b32 s90, v24\n"
      "v_readfirstlane_b32 s91, v25\n"
      "v_readfirstlane_b32 s36, v26\n"
      "v_readfirstlane_b32 s92, v27\n"
      "s_nop 4\n"
      "s_mov_b32 s42, 226492416\n"
      "s_mov_b32 s43, 0x20000\n"
      "s_mov_b32 s47, 0x20000\n"
      "s_mov_b32 s98, 0\n"
      "s_and_b32 s48, s92, 0xff\n"
      "s_bfe_u32 s49, s92, 0x10008\n"
      "s_lshr_b32 s68, s92, 16\n"
      "s_mul_i32 s51, s49, 2621440\n"
      "s_sub_u32 s52, 2621440, s51\n"
      "v_and_b32 v13, 63, v0\n"
      "v_lshlrev_b32 v14, 13, v13\n"
      "s_lshl_b32 s38, s48, 7\n"
      "s_add_u32 s38, s38, 61440\n"
      "v_add_u32 v14, s38, v14\n"
      "v_cmp_eq_u32 vcc, 0, v13\n"
      "v_mov_b32 v12, 41088\n"
      "s_nop 0\n"
      "v_cndmask_b32 v14, v14, v12, vcc\n"
      "v_cmp_lt_u32 vcc, 2, v13\n"
      "v_mov_b32 v12, 1879048192\n"
      "s_nop 0\n"
      "v_cndmask_b32 v14, v14, v12, vcc\n"
      "v_lshlrev_b32 v13, 6, v0\n"
      "s_getpc_b64 s[24:25]\n"
      "pc10_%=:\n"
      "s_add_u32 s24, s24, top1_%=-pc10_%=\n"
      "s_addc_u32 s25, s25, 0\n"
      "s_mov_b32 s26, .Lcode_end-top1_%=\n"
      "s_mov_b32 s27, 0x20000\n"
      "buffer_load_dword v2, v5, s[40:43], 0 offen sc1\n"
      "s_mov_b32 s44, s90\n"
      "s_mov_b32 s45, s91\n"
      "s_mov_b32 s46, 147456\n"
      "s_add_u32 m0, s36, 0\n"
      "s_mov_b32 s38, 0\n"
      "buffer_load_dwordx4 v7, s[44:47], s38 offen lds\n"
      "s_add_u32 m0, s36, 4096\n"
      "s_mov_b32 s38, 18432\n"
      "buffer_load_dwordx4 v7, s[44:47], s38 offen lds\n"
      "s_mov_b32 s44, s86\n"
      "s_mov_b32 s45, s87\n"
      "s_mov_b32 s46, 4718592\n"
      "s_add_u32 m0, s36, 16384\n"
      "s_add_u32 s38, s51, 0\n"
      "buffer_load_dwordx4 v1, s[44:47], s38 offen lds\n"
      "s_add_u32 m0, s36, 20480\n"
      "s_add_u32 s38, s51, 32768\n"
      "buffer_load_dwordx4 v1, s[44:47], s38 offen lds\n"
      "s_add_u32 m0, s36, 24576\n"
      "s_add_u32 s38, s51, 65536\n"
      "buffer_load_dwordx4 v1, s[44:47], s38 offen lds\n"
      "s_add_u32 m0, s36, 28672\n"
      "s_add_u32 s38, s51, 98304\n"
      "buffer_load_dwordx4 v1, s[44:47], s38 offen lds\n"
      "s_add_u32 m0, s36, 32768\n"
      "s_add_u32 s38, s51, 524288\n"
      "buffer_load_dwordx4 v1, s[44:47], s38 offen lds\n"
      "s_add_u32 m0, s36, 36864\n"
      "s_add_u32 s38, s51, 557056\n"
      "buffer_load_dwordx4 v1, s[44:47], s38 offen lds\n"
      "s_add_u32 m0, s36, 40960\n"
      "s_add_u32 s38, s51, 589824\n"
      "buffer_load_dwordx4 v1, s[44:47], s38 offen lds\n"
      "s_add_u32 m0, s36, 45056\n"
      "s_add_u32 s38, s51, 622592\n"
      "buffer_load_dwordx4 v1, s[44:47], s38 offen lds\n"
      "s_add_u32 m0, s36, 49152\n"
      "s_add_u32 s38, s51, 1048576\n"
      "buffer_load_dwordx4 v1, s[44:47], s38 offen lds\n"
      "s_add_u32 m0, s36, 53248\n"
      "s_add_u32 s38, s51, 1081344\n"
      "buffer_load_dwordx4 v1, s[44:47], s38 offen lds\n"
      "s_add_u32 m0, s36, 57344\n"
      "s_add_u32 s38, s51, 1114112\n"
      "buffer_load_dwordx4 v1, s[44:47], s38 offen lds\n"
      "s_add_u32 m0, s36, 61440\n"
      "s_add_u32 s38, s51, 1146880\n"
      "buffer_load_dwordx4 v1, s[44:47], s38 offen lds\n"
      "s_add_u32 m0, s36, 65536\n"
      "s_add_u32 s38, s51, 1572864\n"
      "buffer_load_dwordx4 v1, s[44:47], s38 offen lds\n"
      "s_add_u32 m0, s36, 69632\n"
      "s_add_u32 s38, s51, 1605632\n"
      "buffer_load_dwordx4 v1, s[44:47], s38 offen lds\n"
      "s_add_u32 m0, s36, 73728\n"
      "s_add_u32 s38, s51, 1638400\n"
      "buffer_load_dwordx4 v1, s[44:47], s38 offen lds\n"
      "s_add_u32 m0, s36, 77824\n"
      "s_add_u32 s38, s51, 1671168\n"
      "buffer_load_dwordx4 v1, s[44:47], s38 offen lds\n"
      "s_add_u32 m0, s36, 81920\n"
      "s_add_u32 s38, s52, 0\n"
      "buffer_load_dwordx4 v1, s[44:47], s38 offen lds\n"
      "s_add_u32 m0, s36, 86016\n"
      "s_add_u32 s38, s52, 32768\n"
      "buffer_load_dwordx4 v1, s[44:47], s38 offen lds\n"
      "s_add_u32 m0, s36, 90112\n"
      "s_add_u32 s38, s52, 65536\n"
      "buffer_load_dwordx4 v1, s[44:47], s38 offen lds\n"
      "s_add_u32 m0, s36, 94208\n"
      "s_add_u32 s38, s52, 98304\n"
      "buffer_load_dwordx4 v1, s[44:47], s38 offen lds\n"
      "s_waitcnt vmcnt(20)\n"
      "s_barrier\n"
      "s_add_u32 m0, s36, 98304\n"
      "s_add_u32 s38, s52, 524288\n"
      "buffer_load_dwordx4 v1, s[44:47], s38 offen lds\n"
      "s_add_u32 m0, s36, 102400\n"
      "s_add_u32 s38, s52, 557056\n"
      "buffer_load_dwordx4 v1, s[44:47], s38 offen lds\n"
      "s_add_u32 m0, s36, 106496\n"
      "s_add_u32 s38, s52, 589824\n"
      "buffer_load_dwordx4 v1, s[44:47], s38 offen lds\n"
      "s_add_u32 m0, s36, 110592\n"
      "s_add_u32 s38, s52, 622592\n"
      "buffer_load_dwordx4 v1, s[44:47], s38 offen lds\n"
      "v_readlane_b32 s60, v2, 0\n"
      "v_readlane_b32 s61, v2, 1\n"
      "s_nop 3\n"
      "s_add_u32 s60, s60, 1\n"
      "s_cmp_eq_u32 s60, s61\n"
      "s_addc_u32 s60, s60, 0\n"
      "v_mov_b32 v2, s60\n"
      "s_cmp_eq_u32 s36, 0\n"
      "s_cselect_b32 s61, 0, 1879048192\n"
      "s_cselect_b32 s62, 0, 4\n"
      "s_add_u32 s62, s62, 139264\n"
      "s_mov_b64 exec, 1\n"
      "v_mov_b32 v11, s62\n"
      "ds_write_b32 v11, v2\n"
      "v_add_u32 v11, s61, v5\n"
      "buffer_store_dword v2, v11, s[40:43], 0 offen sc1\n"
      "s_mov_b64 exec, -1\n"
      "v_add_u32 v11, 0, v6\n"
      "ds_read_b64_tr_b16 v[16:17], v11 offset:0\n"
      "ds_read_b64_tr_b16 v[18:19], v11 offset:128\n"
      "ds_read_b64_tr_b16 v[20:21], v11 offset:512\n"
      "ds_read_b64_tr_b16 v[22:23], v11 offset:640\n"
      "s_waitcnt lgkmcnt(0)\n"
      "ds_write_b128 v10, v[16:19]\n"
      "ds_write_b128 v10, v[20:23] offset:1024\n"
      "s_waitcnt lgkmcnt(0)\n"
      "s_waitcnt vmcnt(21)\n"
      "s_barrier\n"
      "s_add_u32 m0, s36, 0\n"
      "s_add_u32 s38, s52, 1048576\n"
      "buffer_load_dwordx4 v1, s[44:47], s38 offen lds\n"
      "s_add_u32 m0, s36, 4096\n"
      "s_add_u32 s38, s52, 1081344\n"
      "buffer_load_dwordx4 v1, s[44:47], s38 offen lds\n"
      "s_add_u32 m0, s36, 8192\n"
      "s_add_u32 s38, s52, 1114112\n"
      "buffer_load_dwordx4 v1, s[44:47], s38 offen lds\n"
      "s_add_u32 m0, s36, 12288\n"
      "s_add_u32 s38, s52, 1146880\n"
      "buffer_load_dwordx4 v1, s[44:47], s38 offen lds\n"
      "v_add_u32 v11, 16384, v3\n"
      "v_add_u32 v12, 16384, v4\n"
      "ds_read_b64_tr_b16 v[24:25], v11 offset:0\n"
      "ds_read_b64_tr_b16 v[26:27], v11 offset:256\n"
      "ds_read_b64_tr_b16 v[28:29], v11 offset:8192\n"
      "ds_read_b64_tr_b16 v[30:31], v11 offset:8448\n"
      "ds_read_b64_tr_b16 v[40:41], v12 offset:0\n"
      "ds_read_b64_tr_b16 v[42:43], v12 offset:256\n"
      "ds_read_b64_tr_b16 v[44:45], v12 offset:8192\n"
      "ds_read_b64_tr_b16 v[46:47], v12 offset:8448\n"
      "s_waitcnt lgkmcnt(0)\n"
      "s_waitcnt vmcnt(21)\n"
      "s_barrier\n"
      "s_add_u32 m0, s36, 16384\n"
      "s_add_u32 s38, s52, 1572864\n"
      "buffer_load_dwordx4 v1, s[44:47], s38 offen lds\n"
      "s_add_u32 m0, s36, 20480\n"
      "s_add_u32 s38, s52, 1605632\n"
      "buffer_load_dwordx4 v1, s[44:47], s38 offen lds\n"
      "s_add_u32 m0, s36, 24576\n"
      "s_add_u32 s38, s52, 1638400\n"
      "buffer_load_dwordx4 v1, s[44:47], s38 offen lds\n"
      "s_add_u32 m0, s36, 28672\n"
      "s_add_u32 s38, s52, 1671168\n"
      "buffer_load_dwordx4 v1, s[44:47], s38 offen lds\n"
      "v_add_u32 v11, 32768, v3\n"
      "v_add_u32 v12, 32768, v4\n"
      "ds_read_b64_tr_b16 v[56:57], v11 offset:0\n"
      "ds_read_b64_tr_b16 v[58:59], v11 offset:256\n"
      "ds_read_b64_tr_b16 v[60:61], v11 offset:8192\n"
      "ds_read_b64_tr_b16 v[62:63], v11 offset:8448\n"
      "ds_read_b64_tr_b16 a[8:9], v12 offset:0\n"
      "ds_read_b64_tr_b16 a[10:11], v12 offset:256\n"
      "ds_read_b64_tr_b16 a[12:13], v12 offset:8192\n"
      "ds_read_b64_tr_b16 a[14:15], v12 offset:8448\n"
      "s_waitcnt lgkmcnt(0)\n"
      "s_waitcnt vmcnt(21)\n"
      "s_barrier\n"
      "s_add_u32 m0, s36, 32768\n"
      "s_mov_b32 s38, 2097152\n"
      "buffer_load_dwordx4 v1, s[44:47], s38 offen lds\n"
      "s_add_u32 m0, s36, 36864\n"
      "s_mov_b32 s38, 2129920\n"
      "buffer_load_dwordx4 v1, s[44:47], s38 offen lds\n"
      "s_add_u32 m0, s36, 40960\n"
      "s_mov_b32 s38, 2162688\n"
      "buffer_load_dwordx4 v1, s[44:47], s38 offen lds\n"
      "s_add_u32 m0, s36, 45056\n"
      "s_mov_b32 s38, 2195456\n"
      "buffer_load_dwordx4 v1, s[44:47], s38 offen lds\n"
      "v_add_u32 v11, 49152, v3\n"
      "v_add_u32 v12, 49152, v4\n"
      "ds_read_b64_tr_b16 a[24:25], v11 offset:0\n"
      "ds_read_b64_tr_b16 a[26:27], v11 offset:256\n"
      "ds_read_b64_tr_b16 a[28:29], v11 offset:8192\n"
      "ds_read_b64_tr_b16 a[30:31], v11 offset:8448\n"
      "ds_read_b64_tr_b16 a[40:41], v12 offset:0\n"
      "ds_read_b64_tr_b16 a[42:43], v12 offset:256\n"
      "ds_read_b64_tr_b16 a[44:45], v12 offset:8192\n"
      "ds_read_b64_tr_b16 a[46:47], v12 offset:8448\n"
      "s_waitcnt lgkmcnt(0)\n"
      "s_waitcnt vmcnt(21)\n"
      "s_barrier\n"
      "s_mov_b32 s44, s88\n"
      "s_mov_b32 s45, s89\n"
      "s_mov_b32 s46, 4718592\n"
      "s_add_u32 m0, s36, 49152\n"
      "s_add_u32 s38, s51, 0\n"
      "buffer_load_dwordx4 v1, s[44:47], s38 offen lds\n"
      "s_add_u32 m0, s36, 53248\n"
      "s_add_u32 s38, s51, 32768\n"
      "buffer_load_dwordx4 v1, s[44:47], s38 offen lds\n"
      "s_add_u32 m0, s36, 57344\n"
      "s_add_u32 s38, s51, 65536\n"
      "buffer_load_dwordx4 v1, s[44:47], s38 offen lds\n"
      "s_add_u32 m0, s36, 61440\n"
      "s_add_u32 s38, s51, 98304\n"
      "buffer_load_dwordx4 v1, s[44:47], s38 offen lds\n"
      "v_add_u32 v11, 65536, v3\n"
      "v_add_u32 v12, 65536, v4\n"
      "ds_read_b64_tr_b16 v[64:65], v11 offset:0\n"
      "ds_read_b64_tr_b16 v[66:67], v11 offset:256\n"
      "ds_read_b64_tr_b16 v[68:69], v11 offset:8192\n"
      "ds_read_b64_tr_b16 v[70:71], v11 offset:8448\n"
      "ds_read_b64_tr_b16 v[80:81], v12 offset:0\n"
      "ds_read_b64_tr_b16 v[82:83], v12 offset:256\n"
      "ds_read_b64_tr_b16 v[84:85], v12 offset:8192\n"
      "ds_read_b64_tr_b16 v[86:87], v12 offset:8448\n"
      "s_waitcnt lgkmcnt(0)\n"
      "s_waitcnt vmcnt(21)\n"
      "s_barrier\n"
      "s_add_u32 m0, s36, 65536\n"
      "s_add_u32 s38, s51, 524288\n"
      "buffer_load_dwordx4 v1, s[44:47], s38 offen lds\n"
      "s_add_u32 m0, s36, 69632\n"
      "s_add_u32 s38, s51, 557056\n"
      "buffer_load_dwordx4 v1, s[44:47], s38 offen lds\n"
      "s_add_u32 m0, s36, 73728\n"
      "s_add_u32 s38, s51, 589824\n"
      "buffer_load_dwordx4 v1, s[44:47], s38 offen lds\n"
      "s_add_u32 m0, s36, 77824\n"
      "s_add_u32 s38, s51, 622592\n"
      "buffer_load_dwordx4 v1, s[44:47], s38 offen lds\n"
      "v_add_u32 v11, 81920, v3\n"
      "v_add_u32 v12, 81920, v4\n"
      "ds_read_b64_tr_b16 a[64:65], v11 offset:0\n"
      "ds_read_b64_tr_b16 a[66:67], v11 offset:256\n"
      "ds_read_b64_tr_b16 a[68:69], v11 offset:8192\n"
      "ds_read_b64_tr_b16 a[70:71], v11 offset:8448\n"
      "ds_read_b64_tr_b16 a[176:177], v12 offset:0\n"
      "ds_read_b64_tr_b16 a[178:179], v12 offset:256\n"
      "ds_read_b64_tr_b16 a[180:181], v12 offset:8192\n"
      "ds_read_b64_tr_b16 a[182:183], v12 offset:8448\n"
      "s_waitcnt lgkmcnt(0)\n"
      "s_waitcnt vmcnt(21)\n"
      "s_barrier\n"
      "s_add_u32 m0, s36, 81920\n"
      "s_add_u32 s38, s51, 1048576\n"
      "buffer_load_dwordx4 v1, s[44:47], s38 offen lds\n"
      "s_add_u32 m0, s36, 86016\n"
      "s_add_u32 s38, s51, 1081344\n"
      "buffer_load_dwordx4 v1, s[44:47], s38 offen lds\n"
      "s_add_u32 m0, s36, 90112\n"
      "s_add_u32 s38, s51, 1114112\n"
      "buffer_load_dwordx4 v1, s[44:47], s38 offen lds\n"
      "s_add_u32 m0, s36, 94208\n"
      "s_add_u32 s38, s51, 1146880\n"
      "buffer_load_dwordx4 v1, s[44:47], s38 offen lds\n"
      "v_add_u32 v11, 98304, v3\n"
      "v_add_u32 v12, 98304, v4\n"
      "ds_read_b64_tr_b16 a[88:89], v11 offset:0\n"
      "ds_read_b64_tr_b16 a[90:91], v11 offset:256\n"
      "ds_read_b64_tr_b16 a[92:93], v11 offset:8192\n"
      "ds_read_b64_tr_b16 a[94:95], v11 offset:8448\n"
      "ds_read_b64_tr_b16 a[184:185], v12 offset:0\n"
      "ds_read_b64_tr_b16 a[186:187], v12 offset:256\n"
      "ds_read_b64_tr_b16 a[188:189], v12 offset:8192\n"
      "ds_read_b64_tr_b16 a[190:191], v12 offset:8448\n"
      "s_waitcnt lgkmcnt(0)\n"
      "s_waitcnt vmcnt(20)\n"
      "s_barrier\n"
      "s_add_u32 m0, s36, 98304\n"
      "s_add_u32 s38, s51, 1572864\n"
      "buffer_load_dwordx4 v1, s[44:47], s38 offen lds\n"
      "s_add_u32 m0, s36, 102400\n"
      "s_add_u32 s38, s51, 1605632\n"
      "buffer_load_dwordx4 v1, s[44:47], s38 offen lds\n"
      "s_add_u32 m0, s36, 106496\n"
      "s_add_u32 s38, s51, 1638400\n"
      "buffer_load_dwordx4 v1, s[44:47], s38 offen lds\n"
      "s_add_u32 m0, s36, 110592\n"
      "s_add_u32 s38, s51, 1671168\n"
      "buffer_load_dwordx4 v1, s[44:47], s38 offen lds\n"
      "v_add_u32 v11, 0, v3\n"
      "v_add_u32 v12, 0, v4\n"
      "ds_read_b64_tr_b16 a[128:129], v11 offset:0\n"
      "ds_read_b64_tr_b16 a[130:131], v11 offset:256\n"
      "ds_read_b64_tr_b16 a[132:133], v11 offset:8192\n"
      "ds_read_b64_tr_b16 a[134:135], v11 offset:8448\n"
      "ds_read_b64_tr_b16 a[192:193], v12 offset:0\n"
      "ds_read_b64_tr_b16 a[194:195], v12 offset:256\n"
      "ds_read_b64_tr_b16 a[196:197], v12 offset:8192\n"
      "ds_read_b64_tr_b16 a[198:199], v12 offset:8448\n"
      "s_waitcnt lgkmcnt(0)\n"
      "s_waitcnt vmcnt(20)\n"
      "s_barrier\n"
      "s_add_u32 m0, s36, 0\n"
      "s_add_u32 s38, s52, 0\n"
      "buffer_load_dwordx4 v1, s[44:47], s38 offen lds\n"
      "s_add_u32 m0, s36, 4096\n"
      "s_add_u32 s38, s52, 32768\n"
      "buffer_load_dwordx4 v1, s[44:47], s38 offen lds\n"
      "s_add_u32 m0, s36, 8192\n"
      "s_add_u32 s38, s52, 65536\n"
      "buffer_load_dwordx4 v1, s[44:47], s38 offen lds\n"
      "s_add_u32 m0, s36, 12288\n"
      "s_add_u32 s38, s52, 98304\n"
      "buffer_load_dwordx4 v1, s[44:47], s38 offen lds\n"
      "v_add_u32 v11, 16384, v3\n"
      "v_add_u32 v12, 16384, v4\n"
      "ds_read_b64_tr_b16 a[136:137], v11 offset:0\n"
      "ds_read_b64_tr_b16 a[138:139], v11 offset:256\n"
      "ds_read_b64_tr_b16 a[140:141], v11 offset:8192\n"
      "ds_read_b64_tr_b16 a[142:143], v11 offset:8448\n"
      "ds_read_b64_tr_b16 a[200:201], v12 offset:0\n"
      "ds_read_b64_tr_b16 a[202:203], v12 offset:256\n"
      "ds_read_b64_tr_b16 a[204:205], v12 offset:8192\n"
      "ds_read_b64_tr_b16 a[206:207], v12 offset:8448\n"
      "s_waitcnt lgkmcnt(0)\n"
      "s_waitcnt vmcnt(20)\n"
      "s_barrier\n"
      "s_add_u32 m0, s36, 16384\n"
      "s_add_u32 s38, s52, 524288\n"
      "buffer_load_dwordx4 v1, s[44:47], s38 offen lds\n"
      "s_add_u32 m0, s36, 20480\n"
      "s_add_u32 s38, s52, 557056\n"
      "buffer_load_dwordx4 v1, s[44:47], s38 offen lds\n"
      "s_add_u32 m0, s36, 24576\n"
      "s_add_u32 s38, s52, 589824\n"
      "buffer_load_dwordx4 v1, s[44:47], s38 offen lds\n"
      "s_add_u32 m0, s36, 28672\n"
      "s_add_u32 s38, s52, 622592\n"
      "buffer_load_dwordx4 v1, s[44:47], s38 offen lds\n"
      "v_add_u32 v11, 32768, v3\n"
      "v_add_u32 v12, 32768, v4\n"
      "ds_read_b64_tr_b16 a[144:145], v11 offset:0\n"
      "ds_read_b64_tr_b16 a[146:147], v11 offset:256\n"
      "ds_read_b64_tr_b16 a[148:149], v11 offset:8192\n"
      "ds_read_b64_tr_b16 a[150:151], v11 offset:8448\n"
      "ds_read_b64_tr_b16 a[208:209], v12 offset:0\n"
      "ds_read_b64_tr_b16 a[210:211], v12 offset:256\n"
      "ds_read_b64_tr_b16 a[212:213], v12 offset:8192\n"
      "ds_read_b64_tr_b16 a[214:215], v12 offset:8448\n"
      "s_waitcnt lgkmcnt(0)\n"
      "s_waitcnt vmcnt(20)\n"
      "s_barrier\n"
      "s_add_u32 m0, s36, 32768\n"
      "s_add_u32 s38, s52, 1048576\n"
      "buffer_load_dwordx4 v1, s[44:47], s38 offen lds\n"
      "s_add_u32 m0, s36, 36864\n"
      "s_add_u32 s38, s52, 1081344\n"
      "buffer_load_dwordx4 v1, s[44:47], s38 offen lds\n"
      "s_add_u32 m0, s36, 40960\n"
      "s_add_u32 s38, s52, 1114112\n"
      "buffer_load_dwordx4 v1, s[44:47], s38 offen lds\n"
      "s_add_u32 m0, s36, 45056\n"
      "s_add_u32 s38, s52, 1146880\n"
      "buffer_load_dwordx4 v1, s[44:47], s38 offen lds\n"
      "v_add_u32 v11, 49152, v3\n"
      "v_add_u32 v12, 49152, v4\n"
      "ds_read_b64_tr_b16 a[216:217], v11 offset:0\n"
      "ds_read_b64_tr_b16 a[218:219], v11 offset:256\n"
      "ds_read_b64_tr_b16 a[220:221], v11 offset:8192\n"
      "ds_read_b64_tr_b16 a[222:223], v11 offset:8448\n"
      "ds_read_b64_tr_b16 a[224:225], v12 offset:0\n"
      "ds_read_b64_tr_b16 a[226:227], v12 offset:256\n"
      "ds_read_b64_tr_b16 a[228:229], v12 offset:8192\n"
      "ds_read_b64_tr_b16 a[230:231], v12 offset:8448\n"
      "s_waitcnt lgkmcnt(0)\n"
      "s_waitcnt vmcnt(20)\n"
      "s_barrier\n"
      "s_add_u32 m0, s36, 49152\n"
      "s_add_u32 s38, s52, 1572864\n"
      "buffer_load_dwordx4 v1, s[44:47], s38 offen lds\n"
      "s_add_u32 m0, s36, 53248\n"
      "s_add_u32 s38, s52, 1605632\n"
      "buffer_load_dwordx4 v1, s[44:47], s38 offen lds\n"
      "s_add_u32 m0, s36, 57344\n"
      "s_add_u32 s38, s52, 1638400\n"
      "buffer_load_dwordx4 v1, s[44:47], s38 offen lds\n"
      "s_add_u32 m0, s36, 61440\n"
      "s_add_u32 s38, s52, 1671168\n"
      "buffer_load_dwordx4 v1, s[44:47], s38 offen lds\n"
      "v_mov_b32 v11, 139264\n"
      "ds_read_b32 v11, v11\n"
      "buffer_load_dwordx2 v[6:7], v5, s[40:43], 0 offen sc1\n"
      "s_waitcnt lgkmcnt(0)\n"
      "v_readfirstlane_b32 s60, v11\n"
      "v_add_u32 v11, 65536, v3\n"
      "v_add_u32 v12, 65536, v4\n"
      "ds_read_b64_tr_b16 a[232:233], v11 offset:0\n"
      "ds_read_b64_tr_b16 a[234:235], v11 offset:256\n"
      "ds_read_b64_tr_b16 a[236:237], v11 offset:8192\n"
      "ds_read_b64_tr_b16 a[238:239], v11 offset:8448\n"
      "ds_read_b64_tr_b16 a[240:241], v12 offset:0\n"
      "ds_read_b64_tr_b16 a[242:243], v12 offset:256\n"
      "ds_read_b64_tr_b16 a[244:245], v12 offset:8192\n"
      "ds_read_b64_tr_b16 a[246:247], v12 offset:8448\n"
      "s_waitcnt lgkmcnt(0)\n"
      "s_waitcnt vmcnt(21)\n"
      "s_barrier\n"
      "s_add_u32 m0, s36, 65536\n"
      "s_mov_b32 s38, 2097152\n"
      "buffer_load_dwordx4 v1, s[44:47], s38 offen lds\n"
      "s_add_u32 m0, s36, 69632\n"
      "s_mov_b32 s38, 2129920\n"
      "buffer_load_dwordx4 v1, s[44:47], s38 offen lds\n"
      "s_add_u32 m0, s36, 73728\n"
      "s_mov_b32 s38, 2162688\n"
      "buffer_load_dwordx4 v1, s[44:47], s38 offen lds\n"
      "s_add_u32 m0, s36, 77824\n"
      "s_mov_b32 s38, 2195456\n"
      "buffer_load_dwordx4 v1, s[44:47], s38 offen lds\n"
      "v_add_u32 v11, 81920, v3\n"
      "v_add_u32 v12, 81920, v4\n"
      "ds_read_b64_tr_b16 a[248:249], v11 offset:0\n"
      "ds_read_b64_tr_b16 a[250:251], v11 offset:256\n"
      "ds_read_b64_tr_b16 a[252:253], v11 offset:8192\n"
      "ds_read_b64_tr_b16 a[254:255], v11 offset:8448\n"
      "ds_read_b64_tr_b16 v[120:121], v12 offset:0\n"
      "ds_read_b64_tr_b16 v[122:123], v12 offset:256\n"
      "ds_read_b64_tr_b16 v[124:125], v12 offset:8192\n"
      "ds_read_b64_tr_b16 v[126:127], v12 offset:8448\n"
      "s_waitcnt lgkmcnt(0)\n"
      "s_waitcnt vmcnt(21)\n"
      "s_barrier\n"
      "s_mov_b32 s44, s84\n"
      "s_mov_b32 s45, s85\n"
      "s_mov_b32 s46, 4718592\n"
      "s_add_u32 m0, s36, 81920\n"
      "s_add_u32 s38, s51, 0\n"
      "buffer_load_dwordx4 v1, s[44:47], s38 offen lds\n"
      "s_add_u32 m0, s36, 86016\n"
      "s_add_u32 s38, s51, 32768\n"
      "buffer_load_dwordx4 v1, s[44:47], s38 offen lds\n"
      "s_add_u32 m0, s36, 90112\n"
      "s_add_u32 s38, s51, 65536\n"
      "buffer_load_dwordx4 v1, s[44:47], s38 offen lds\n"
      "s_add_u32 m0, s36, 94208\n"
      "s_add_u32 s38, s51, 98304\n"
      "buffer_load_dwordx4 v1, s[44:47], s38 offen lds\n"
      "v_add_u32 v11, 98304, v3\n"
      "v_add_u32 v12, 98304, v4\n"
      "ds_read_b64_tr_b16 v[128:129], v11 offset:0\n"
      "ds_read_b64_tr_b16 v[130:131], v11 offset:256\n"
      "ds_read_b64_tr_b16 v[132:133], v11 offset:8192\n"
      "ds_read_b64_tr_b16 v[134:135], v11 offset:8448\n"
      "ds_read_b64_tr_b16 v[136:137], v12 offset:0\n"
      "ds_read_b64_tr_b16 v[138:139], v12 offset:256\n"
      "ds_read_b64_tr_b16 v[140:141], v12 offset:8192\n"
      "ds_read_b64_tr_b16 v[142:143], v12 offset:8448\n"
      "s_waitcnt lgkmcnt(0)\n"
      "s_waitcnt vmcnt(21)\n"
      "s_barrier\n"
      "s_add_u32 m0, s36, 98304\n"
      "s_add_u32 s38, s51, 524288\n"
      "buffer_load_dwordx4 v1, s[44:47], s38 offen lds\n"
      "s_add_u32 m0, s36, 102400\n"
      "s_add_u32 s38, s51, 557056\n"
      "buffer_load_dwordx4 v1, s[44:47], s38 offen lds\n"
      "s_add_u32 m0, s36, 106496\n"
      "s_add_u32 s38, s51, 589824\n"
      "buffer_load_dwordx4 v1, s[44:47], s38 offen lds\n"
      "s_add_u32 m0, s36, 110592\n"
      "s_add_u32 s38, s51, 622592\n"
      "buffer_load_dwordx4 v1, s[44:47], s38 offen lds\n"
      "v_add_u32 v11, 0, v3\n"
      "v_add_u32 v12, 0, v4\n"
      "ds_read_b64_tr_b16 v[144:145], v11 offset:0\n"
      "ds_read_b64_tr_b16 v[146:147], v11 offset:256\n"
      "ds_read_b64_tr_b16 v[148:149], v11 offset:8192\n"
      "ds_read_b64_tr_b16 v[150:151], v11 offset:8448\n"
      "ds_read_b64_tr_b16 v[152:153], v12 offset:0\n"
      "ds_read_b64_tr_b16 v[154:155], v12 offset:256\n"
      "ds_read_b64_tr_b16 v[156:157], v12 offset:8192\n"
      "ds_read_b64_tr_b16 v[158:159], v12 offset:8448\n"
      "s_waitcnt lgkmcnt(0)\n"
      "s_waitcnt vmcnt(21)\n"
      "s_barrier\n"
      "s_add_u32 m0, s36, 0\n"
      "s_add_u32 s38, s51, 1048576\n"
      "buffer_load_dwordx4 v1, s[44:47], s38 offen lds\n"
      "s_add_u32 m0, s36, 4096\n"
      "s_add_u32 s38, s51, 1081344\n"
      "buffer_load_dwordx4 v1, s[44:47], s38 offen lds\n"
      "s_add_u32 m0, s36, 8192\n"
      "s_add_u32 s38, s51, 1114112\n"
      "buffer_load_dwordx4 v1, s[44:47], s38 offen lds\n"
      "s_add_u32 m0, s36, 12288\n"
      "s_add_u32 s38, s51, 1146880\n"
      "buffer_load_dwordx4 v1, s[44:47], s38 offen lds\n"
      "v_add_u32 v11, 16384, v3\n"
      "v_add_u32 v12, 16384, v4\n"
      "ds_read_b64_tr_b16 v[160:161], v11 offset:0\n"
      "ds_read_b64_tr_b16 v[162:163], v11 offset:256\n"
      "ds_read_b64_tr_b16 v[164:165], v11 offset:8192\n"
      "ds_read_b64_tr_b16 v[166:167], v11 offset:8448\n"
      "ds_read_b64_tr_b16 v[168:169], v12 offset:0\n"
      "ds_read_b64_tr_b16 v[170:171], v12 offset:256\n"
      "ds_read_b64_tr_b16 v[172:173], v12 offset:8192\n"
      "ds_read_b64_tr_b16 v[174:175], v12 offset:8448\n"
      "s_waitcnt lgkmcnt(0)\n"
      "s_waitcnt vmcnt(21)\n"
      "s_barrier\n"
      "s_add_u32 m0, s36, 16384\n"
      "s_add_u32 s38, s51, 1572864\n"
      "buffer_load_dwordx4 v1, s[44:47], s38 offen lds\n"
      "s_add_u32 m0, s36, 20480\n"
      "s_add_u32 s38, s51, 1605632\n"
      "buffer_load_dwordx4 v1, s[44:47], s38 offen lds\n"
      "s_add_u32 m0, s36, 24576\n"
      "s_add_u32 s38, s51, 1638400\n"
      "buffer_load_dwordx4 v1, s[44:47], s38 offen lds\n"
      "s_add_u32 m0, s36, 28672\n"
      "s_add_u32 s38, s51, 1671168\n"
      "buffer_load_dwordx4 v1, s[44:47], s38 offen lds\n"
      "v_add_u32 v11, 32768, v3\n"
      "v_add_u32 v12, 32768, v4\n"
      "ds_read_b64_tr_b16 v[176:177], v11 offset:0\n"
      "ds_read_b64_tr_b16 v[178:179], v11 offset:256\n"
      "ds_read_b64_tr_b16 v[180:181], v11 offset:8192\n"
      "ds_read_b64_tr_b16 v[182:183], v11 offset:8448\n"
      "ds_read_b64_tr_b16 v[184:185], v12 offset:0\n"
      "ds_read_b64_tr_b16 v[186:187], v12 offset:256\n"
      "ds_read_b64_tr_b16 v[188:189], v12 offset:8192\n"
      "ds_read_b64_tr_b16 v[190:191], v12 offset:8448\n"
      "s_waitcnt lgkmcnt(0)\n"
      "s_waitcnt vmcnt(21)\n"
      "s_barrier\n"
      "s_add_u32 m0, s36, 32768\n"
      "s_add_u32 s38, s52, 0\n"
      "buffer_load_dwordx4 v1, s[44:47], s38 offen lds\n"
      "s_add_u32 m0, s36, 36864\n"
      "s_add_u32 s38, s52, 32768\n"
      "buffer_load_dwordx4 v1, s[44:47], s38 offen lds\n"
      "s_add_u32 m0, s36, 40960\n"
      "s_add_u32 s38, s52, 65536\n"
      "buffer_load_dwordx4 v1, s[44:47], s38 offen lds\n"
      "s_add_u32 m0, s36, 45056\n"
      "s_add_u32 s38, s52, 98304\n"
      "buffer_load_dwordx4 v1, s[44:47], s38 offen lds\n"
      "v_add_u32 v11, 49152, v3\n"
      "v_add_u32 v12, 49152, v4\n"
      "ds_read_b64_tr_b16 v[192:193], v11 offset:0\n"
      "ds_read_b64_tr_b16 v[194:195], v11 offset:256\n"
      "ds_read_b64_tr_b16 v[196:197], v11 offset:8192\n"
      "ds_read_b64_tr_b16 v[198:199], v11 offset:8448\n"
      "ds_read_b64_tr_b16 v[200:201], v12 offset:0\n"
      "ds_read_b64_tr_b16 v[202:203], v12 offset:256\n"
      "ds_read_b64_tr_b16 v[204:205], v12 offset:8192\n"
      "ds_read_b64_tr_b16 v[206:207], v12 offset:8448\n"
      "s_waitcnt lgkmcnt(0)\n"
      "s_waitcnt vmcnt(20)\n"
      "s_barrier\n"
      "s_add_u32 m0, s36, 49152\n"
      "s_add_u32 s38, s52, 524288\n"
      "buffer_load_dwordx4 v1, s[44:47], s38 offen lds\n"
      "s_add_u32 m0, s36, 53248\n"
      "s_add_u32 s38, s52, 557056\n"
      "buffer_load_dwordx4 v1, s[44:47], s38 offen lds\n"
      "s_add_u32 m0, s36, 57344\n"
      "s_add_u32 s38, s52, 589824\n"
      "buffer_load_dwordx4 v1, s[44:47], s38 offen lds\n"
      "s_add_u32 m0, s36, 61440\n"
      "s_add_u32 s38, s52, 622592\n"
      "buffer_load_dwordx4 v1, s[44:47], s38 offen lds\n"
      "v_readlane_b32 s61, v6, 1\n"
      "v_readlane_b32 s62, v7, 1\n"
      "s_nop 3\n"
      "s_cmp_eq_u32 s60, s61\n"
      "s_cselect_b32 s63, 1, 0\n"
      "s_cselect_b32 s61, s36, 1\n"
      "s_cmp_eq_u32 s61, 0\n"
      "s_cselect_b32 s61, 4, 1879048192\n"
      "s_mov_b64 exec, 1\n"
      "v_mov_b32 v6, s62\n"
      "v_add_u32 v11, s61, v5\n"
      "buffer_store_dword v6, v11, s[40:43], 0 offen sc1\n"
      "s_mov_b64 exec, -1\n"
      "v_add_u32 v11, 65536, v3\n"
      "v_add_u32 v12, 65536, v4\n"
      "ds_read_b64_tr_b16 v[208:209], v11 offset:0\n"
      "ds_read_b64_tr_b16 v[210:211], v11 offset:256\n"
      "ds_read_b64_tr_b16 v[212:213], v11 offset:8192\n"
      "ds_read_b64_tr_b16 v[214:215], v11 offset:8448\n"
      "ds_read_b64_tr_b16 v[216:217], v12 offset:0\n"
      "ds_read_b64_tr_b16 v[218:219], v12 offset:256\n"
      "ds_read_b64_tr_b16 v[220:221], v12 offset:8192\n"
      "ds_read_b64_tr_b16 v[222:223], v12 offset:8448\n"
      "s_waitcnt lgkmcnt(0)\n"
      "s_waitcnt vmcnt(21)\n"
      "s_barrier\n"
      "s_add_u32 m0, s36, 65536\n"
      "s_add_u32 s38, s52, 1048576\n"
      "buffer_load_dwordx4 v1, s[44:47], s38 offen lds\n"
      "s_add_u32 m0, s36, 69632\n"
      "s_add_u32 s38, s52, 1081344\n"
      "buffer_load_dwordx4 v1, s[44:47], s38 offen lds\n"
      "s_add_u32 m0, s36, 73728\n"
      "s_add_u32 s38, s52, 1114112\n"
      "buffer_load_dwordx4 v1, s[44:47], s38 offen lds\n"
      "s_add_u32 m0, s36, 77824\n"
      "s_add_u32 s38, s52, 1146880\n"
      "buffer_load_dwordx4 v1, s[44:47], s38 offen lds\n"
      "v_add_u32 v11, 81920, v3\n"
      "v_add_u32 v12, 81920, v4\n"
      "ds_read_b64_tr_b16 v[16:17], v11 offset:0\n"
      "ds_read_b64_tr_b16 v[18:19], v11 offset:256\n"
      "ds_read_b64_tr_b16 v[20:21], v11 offset:8192\n"
      "ds_read_b64_tr_b16 v[22:23], v11 offset:8448\n"
      "ds_read_b64_tr_b16 v[32:33], v12 offset:0\n"
      "ds_read_b64_tr_b16 v[34:35], v12 offset:256\n"
      "ds_read_b64_tr_b16 v[36:37], v12 offset:8192\n"
      "ds_read_b64_tr_b16 v[38:39], v12 offset:8448\n"
      "s_waitcnt lgkmcnt(0)\n"
      "s_waitcnt vmcnt(21)\n"
      "s_barrier\n"
      "s_add_u32 m0, s36, 81920\n"
      "s_add_u32 s38, s52, 1572864\n"
      "buffer_load_dwordx4 v1, s[44:47], s38 offen lds\n"
      "s_add_u32 m0, s36, 86016\n"
      "s_add_u32 s38, s52, 1605632\n"
      "buffer_load_dwordx4 v1, s[44:47], s38 offen lds\n"
      "s_add_u32 m0, s36, 90112\n"
      "s_add_u32 s38, s52, 1638400\n"
      "buffer_load_dwordx4 v1, s[44:47], s38 offen lds\n"
      "s_add_u32 m0, s36, 94208\n"
      "s_add_u32 s38, s52, 1671168\n"
      "buffer_load_dwordx4 v1, s[44:47], s38 offen lds\n"
      "v_add_u32 v11, 98304, v3\n"
      "v_add_u32 v12, 98304, v4\n"
      "ds_read_b64_tr_b16 v[48:49], v11 offset:0\n"
      "ds_read_b64_tr_b16 v[50:51], v11 offset:256\n"
      "ds_read_b64_tr_b16 v[52:53], v11 offset:8192\n"
      "ds_read_b64_tr_b16 v[54:55], v11 offset:8448\n"
      "ds_read_b64_tr_b16 a[0:1], v12 offset:0\n"
      "ds_read_b64_tr_b16 a[2:3], v12 offset:256\n"
      "ds_read_b64_tr_b16 a[4:5], v12 offset:8192\n"
      "ds_read_b64_tr_b16 a[6:7], v12 offset:8448\n"
      "s_waitcnt lgkmcnt(0)\n"
      "s_waitcnt vmcnt(21)\n"
      "s_barrier\n"
      "s_add_u32 m0, s36, 98304\n"
      "s_mov_b32 s38, 2097152\n"
      "buffer_load_dwordx4 v1, s[44:47], s38 offen lds\n"
      "s_add_u32 m0, s36, 102400\n"
      "s_mov_b32 s38, 2129920\n"
      "buffer_load_dwordx4 v1, s[44:47], s38 offen lds\n"
      "s_add_u32 m0, s36, 106496\n"
      "s_mov_b32 s38, 2162688\n"
      "buffer_load_dwordx4 v1, s[44:47], s38 offen lds\n"
      "s_add_u32 m0, s36, 110592\n"
      "s_mov_b32 s38, 2195456\n"
      "buffer_load_dwordx4 v1, s[44:47], s38 offen lds\n"
      "v_add_u32 v11, 0, v3\n"
      "v_add_u32 v12, 0, v4\n"
      "ds_read_b64_tr_b16 a[16:17], v11 offset:0\n"
      "ds_read_b64_tr_b16 a[18:19], v11 offset:256\n"
      "ds_read_b64_tr_b16 a[20:21], v11 offset:8192\n"
      "ds_read_b64_tr_b16 a[22:23], v11 offset:8448\n"
      "ds_read_b64_tr_b16 a[32:33], v12 offset:0\n"
      "ds_read_b64_tr_b16 a[34:35], v12 offset:256\n"
      "ds_read_b64_tr_b16 a[36:37], v12 offset:8192\n"
      "ds_read_b64_tr_b16 a[38:39], v12 offset:8448\n"
      "s_waitcnt lgkmcnt(0)\n"
      "s_waitcnt vmcnt(21)\n"
      "s_barrier\n"
      "s_mov_b32 s44, s90\n"
      "s_mov_b32 s45, s91\n"
      "s_mov_b32 s46, 147456\n"
      "s_add_u32 m0, s36, 0\n"
      "s_mov_b32 s38, 0\n"
      "buffer_load_dwordx4 v9, s[44:47], s38 offen lds\n"
      "s_add_u32 m0, s36, 4096\n"
      "s_mov_b32 s38, 9216\n"
      "buffer_load_dwordx4 v9, s[44:47], s38 offen lds\n"
      "s_add_u32 m0, s36, 8192\n"
      "s_mov_b32 s38, 18432\n"
      "buffer_load_dwordx4 v9, s[44:47], s38 offen lds\n"
      "s_add_u32 m0, s36, 12288\n"
      "s_mov_b32 s38, 27648\n"
      "buffer_load_dwordx4 v9, s[44:47], s38 offen lds\n"
      "v_add_u32 v11, 16384, v3\n"
      "v_add_u32 v12, 16384, v4\n"
      "ds_read_b64_tr_b16 a[48:49], v11 offset:0\n"
      "ds_read_b64_tr_b16 a[50:51], v11 offset:256\n"
      "ds_read_b64_tr_b16 a[52:53], v11 offset:8192\n"
      "ds_read_b64_tr_b16 a[54:55], v11 offset:8448\n"
      "ds_read_b64_tr_b16 v[72:73], v12 offset:0\n"
      "ds_read_b64_tr_b16 v[74:75], v12 offset:256\n"
      "ds_read_b64_tr_b16 v[76:77], v12 offset:8192\n"
      "ds_read_b64_tr_b16 v[78:79], v12 offset:8448\n"
      "s_waitcnt lgkmcnt(0)\n"
      "s_waitcnt vmcnt(21)\n"
      "s_barrier\n"
      "s_add_u32 m0, s36, 16384\n"
      "s_mov_b32 s38, 36864\n"
      "buffer_load_dwordx4 v9, s[44:47], s38 offen lds\n"
      "s_add_u32 m0, s36, 20480\n"
      "s_mov_b32 s38, 46080\n"
      "buffer_load_dwordx4 v9, s[44:47], s38 offen lds\n"
      "s_add_u32 m0, s36, 24576\n"
      "s_mov_b32 s38, 55296\n"
      "buffer_load_dwordx4 v9, s[44:47], s38 offen lds\n"
      "s_add_u32 m0, s36, 28672\n"
      "s_mov_b32 s38, 64512\n"
      "buffer_load_dwordx4 v9, s[44:47], s38 offen lds\n"
      "buffer_load_dword v13, v13, s[24:27], 0 offen\n"
      "v_add_u32 v11, 32768, v3\n"
      "v_add_u32 v12, 32768, v4\n"
      "ds_read_b64_tr_b16 a[56:57], v11 offset:0\n"
      "ds_read_b64_tr_b16 a[58:59], v11 offset:256\n"
      "ds_read_b64_tr_b16 a[60:61], v11 offset:8192\n"
      "ds_read_b64_tr_b16 a[62:63], v11 offset:8448\n"
      "ds_read_b64_tr_b16 a[72:73], v12 offset:0\n"
      "ds_read_b64_tr_b16 a[74:75], v12 offset:256\n"
      "ds_read_b64_tr_b16 a[76:77], v12 offset:8192\n"
      "ds_read_b64_tr_b16 a[78:79], v12 offset:8448\n"
      "s_waitcnt lgkmcnt(0)\n"
      "s_waitcnt vmcnt(22)\n"
      "s_barrier\n"
      "s_add_u32 m0, s36, 32768\n"
      "s_mov_b32 s38, 73728\n"
      "buffer_load_dwordx4 v9, s[44:47], s38 offen lds\n"
      "s_add_u32 m0, s36, 36864\n"
      "s_mov_b32 s38, 82944\n"
      "buffer_load_dwordx4 v9, s[44:47], s38 offen lds\n"
      "s_add_u32 m0, s36, 40960\n"
      "s_mov_b32 s38, 92160\n"
      "buffer_load_dwordx4 v9, s[44:47], s38 offen lds\n"
      "s_add_u32 m0, s36, 45056\n"
      "s_mov_b32 s38, 101376\n"
      "buffer_load_dwordx4 v9, s[44:47], s38 offen lds\n"
      "v_add_u32 v11, 49152, v3\n"
      "v_add_u32 v12, 49152, v4\n"
      "ds_read_b64_tr_b16 a[80:81], v11 offset:0\n"
      "ds_read_b64_tr_b16 a[82:83], v11 offset:256\n"
      "ds_read_b64_tr_b16 a[84:85], v11 offset:8192\n"
      "ds_read_b64_tr_b16 a[86:87], v11 offset:8448\n"
      "ds_read_b64_tr_b16 a[96:97], v12 offset:0\n"
      "ds_read_b64_tr_b16 a[98:99], v12 offset:256\n"
      "ds_read_b64_tr_b16 a[100:101], v12 offset:8192\n"
      "ds_read_b64_tr_b16 a[102:103], v12 offset:8448\n"
      "s_waitcnt lgkmcnt(0)\n"
      "s_waitcnt vmcnt(21)\n"
      "s_barrier\n"
      "s_add_u32 m0, s36, 49152\n"
      "s_mov_b32 s38, 110592\n"
      "buffer_load_dwordx4 v9, s[44:47], s38 offen lds\n"
      "s_add_u32 m0, s36, 53248\n"
      "s_mov_b32 s38, 119808\n"
      "buffer_load_dwordx4 v9, s[44:47], s38 offen lds\n"
      "s_add_u32 m0, s36, 57344\n"
      "s_mov_b32 s38, 129024\n"
      "buffer_load_dwordx4 v9, s[44:47], s38 offen lds\n"
      "s_add_u32 m0, s36, 61440\n"
      "s_mov_b32 s38, 138240\n"
      "buffer_load_dwordx4 v9, s[44:47], s38 offen lds\n"
      "buffer_load_dword v0, v14, s[40:43], 0 offen sc1\n"
      "v_add_u32 v11, 65536, v3\n"
      "v_add_u32 v12, 65536, v4\n"
      "ds_read_b64_tr_b16 a[104:105], v11 offset:0\n"
      "ds_read_b64_tr_b16 a[106:107], v11 offset:256\n"
      "ds_read_b64_tr_b16 a[108:109], v11 offset:8192\n"
      "ds_read_b64_tr_b16 a[110:111], v11 offset:8448\n"
      "ds_read_b64_tr_b16 a[152:153], v12 offset:0\n"
      "ds_read_b64_tr_b16 a[154:155], v12 offset:256\n"
      "ds_read_b64_tr_b16 a[156:157], v12 offset:8192\n"
      "ds_read_b64_tr_b16 a[158:159], v12 offset:8448\n"
      "s_waitcnt lgkmcnt(0)\n"
      "s_waitcnt vmcnt(22)\n"
      "s_barrier\n"
      "v_add_u32 v11, 81920, v3\n"
      "v_add_u32 v12, 81920, v4\n"
      "ds_read_b64_tr_b16 a[112:113], v11 offset:0\n"
      "ds_read_b64_tr_b16 a[114:115], v11 offset:256\n"
      "ds_read_b64_tr_b16 a[116:117], v11 offset:8192\n"
      "ds_read_b64_tr_b16 a[118:119], v11 offset:8448\n"
      "ds_read_b64_tr_b16 a[160:161], v12 offset:0\n"
      "ds_read_b64_tr_b16 a[162:163], v12 offset:256\n"
      "ds_read_b64_tr_b16 a[164:165], v12 offset:8192\n"
      "ds_read_b64_tr_b16 a[166:167], v12 offset:8448\n"
      "s_waitcnt lgkmcnt(0)\n"
      "s_waitcnt vmcnt(18)\n"
      "s_barrier\n"
      "v_add_u32 v11, 98304, v3\n"
      "v_add_u32 v12, 98304, v4\n"
      "ds_read_b64_tr_b16 a[120:121], v11 offset:0\n"
      "ds_read_b64_tr_b16 a[122:123], v11 offset:256\n"
      "ds_read_b64_tr_b16 a[124:125], v11 offset:8192\n"
      "ds_read_b64_tr_b16 a[126:127], v11 offset:8448\n"
      "ds_read_b64_tr_b16 a[168:169], v12 offset:0\n"
      "ds_read_b64_tr_b16 a[170:171], v12 offset:256\n"
      "ds_read_b64_tr_b16 a[172:173], v12 offset:8192\n"
      "ds_read_b64_tr_b16 a[174:175], v12 offset:8448\n"
      "s_waitcnt lgkmcnt(0)\n"
      "s_waitcnt vmcnt(14)\n"
      "s_barrier\n"
      "v_add_u32 v11, 0, v8\n"
      "ds_read_b64_tr_b16 v[224:225], v11 offset:0\n"
      "ds_read_b64_tr_b16 v[226:227], v11 offset:256\n"
      "ds_read_b64_tr_b16 v[228:229], v11 offset:4096\n"
      "ds_read_b64_tr_b16 v[230:231], v11 offset:4352\n"
      "ds_read_b64_tr_b16 v[88:89], v11 offset:64\n"
      "ds_read_b64_tr_b16 v[90:91], v11 offset:320\n"
      "ds_read_b64_tr_b16 v[92:93], v11 offset:4160\n"
      "ds_read_b64_tr_b16 v[94:95], v11 offset:4416\n"
      "s_waitcnt lgkmcnt(0)\n"
      "s_waitcnt vmcnt(10)\n"
      "s_barrier\n"
      "v_add_u32 v11, 16384, v8\n"
      "ds_read_b64_tr_b16 v[232:233], v11 offset:0\n"
      "ds_read_b64_tr_b16 v[234:235], v11 offset:256\n"
      "ds_read_b64_tr_b16 v[236:237], v11 offset:4096\n"
      "ds_read_b64_tr_b16 v[238:239], v11 offset:4352\n"
      "ds_read_b64_tr_b16 v[96:97], v11 offset:64\n"
      "ds_read_b64_tr_b16 v[98:99], v11 offset:320\n"
      "ds_read_b64_tr_b16 v[100:101], v11 offset:4160\n"
      "ds_read_b64_tr_b16 v[102:103], v11 offset:4416\n"
      "s_waitcnt lgkmcnt(0)\n"
      "s_waitcnt vmcnt(5)\n"
      "s_barrier\n"
      "v_add_u32 v11, 32768, v8\n"
      "ds_read_b64_tr_b16 v[240:241], v11 offset:0\n"
      "ds_read_b64_tr_b16 v[242:243], v11 offset:256\n"
      "ds_read_b64_tr_b16 v[244:245], v11 offset:4096\n"
      "ds_read_b64_tr_b16 v[246:247], v11 offset:4352\n"
      "ds_read_b64_tr_b16 v[104:105], v11 offset:64\n"
      "ds_read_b64_tr_b16 v[106:107], v11 offset:320\n"
      "ds_read_b64_tr_b16 v[108:109], v11 offset:4160\n"
      "ds_read_b64_tr_b16 v[110:111], v11 offset:4416\n"
      "s_waitcnt lgkmcnt(0)\n"
      "s_waitcnt vmcnt(1)\n"
      "s_barrier\n"
      "v_add_u32 v11, 49152, v8\n"
      "ds_read_b64_tr_b16 v[248:249], v11 offset:0\n"
      "ds_read_b64_tr_b16 v[250:251], v11 offset:256\n"
      "ds_read_b64_tr_b16 v[252:253], v11 offset:4096\n"
      "ds_read_b64_tr_b16 v[254:255], v11 offset:4352\n"
      "ds_read_b64_tr_b16 v[112:113], v11 offset:64\n"
      "ds_read_b64_tr_b16 v[114:115], v11 offset:320\n"
      "ds_read_b64_tr_b16 v[116:117], v11 offset:4160\n"
      "ds_read_b64_tr_b16 v[118:119], v11 offset:4416\n"
      "s_waitcnt lgkmcnt(0)\n"
      "s_cmp_eq_u32 s63, 1\n"
      "s_cbranch_scc1 hd12_%=\n"
      "hs11_%=:\n"
      "s_sleep 2\n"
      "buffer_load_dwordx2 v[6:7], v5, s[40:43], 0 offen sc1\n"
      "s_waitcnt vmcnt(0)\n"
      "v_readlane_b32 s61, v6, 1\n"
      "v_readlane_b32 s62, v7, 1\n"
      "s_nop 3\n"
      "s_cmp_eq_u32 s60, s61\n"
      "s_cselect_b32 s63, 1, 0\n"
      "s_cselect_b32 s61, s36, 1\n"
      "s_cmp_eq_u32 s61, 0\n"
      "s_cselect_b32 s61, 4, 1879048192\n"
      "s_mov_b64 exec, 1\n"
      "v_mov_b32 v6, s62\n"
      "v_add_u32 v11, s61, v5\n"
      "buffer_store_dword v6, v11, s[40:43], 0 offen sc1\n"
      "s_mov_b64 exec, -1\n"
      "s_add_u32 s98, s98, 1\n"
      "s_cmp_ge_u32 s98, 524288\n"
      "s_cselect_b32 s63, 1, s63\n"
      "s_cmp_eq_u32 s63, 1\n"
      "s_cbranch_scc0 hs11_%=\n"
      "s_waitcnt vmcnt(0)\n"
      "buffer_load_dword v0, v14, s[40:43], 0 offen sc1\n"
      "hd12_%=:\n"
      "pw13_%=:\n"
      "s_waitcnt vmcnt(0)\n"
      "v_readlane_b32 s22, v0, 0\n"
      "v_readlane_b32 s84, v0, 1\n"
      "v_readlane_b32 s85, v0, 2\n"
      "s_nop 3\n"
      "s_bitcmp1_b32 s22, 13\n"
      "s_cselect_b32 s84, s85, s84\n"
      "s_cmp_lt_i32 s22, 0\n"
      "s_cselect_b32 s84, -1, s84\n"
      "s_cmp_ge_i32 s84, 2\n"
      "s_cbranch_scc1 pg14_%=\n"
      "s_add_u32 s98, s98, 1\n"
      "s_cmp_ge_u32 s98, 524288\n"
      "s_cbranch_scc1 pg14_%=\n"
      "s_sleep 2\n"
      "buffer_load_dword v0, v14, s[40:43], 0 offen sc1\n"
      "s_waitcnt vmcnt(0)\n"
      "s_branch pw13_%=\n"
      "pg14_%=:\n"
      "s_and_b32 s50, s22, 8192\n"
      "s_and_b32 s22, s22, 24576\n"
      "s_add_u32 s22, s22, 131072\n"
      "ds_read_b32 v0, v15 offset:10240\n"
      "ds_read_b32 v1, v15 offset:11264\n"
      "ds_read_b32 v2, v15 offset:12288\n"
      "ds_read_b32 v3, v15 offset:13312\n"
      "ds_read_b32 v4, v15 offset:14336\n"
      "ds_read_b32 v5, v15 offset:15360\n"
      "ds_read_b32 v6, v15 offset:16384\n"
      "ds_read_b32 v7, v15 offset:17408\n"
      "ds_read_b32 v8, v15 offset:18432\n"
      "ds_read_b32 v9, v15 offset:19456\n"
      "ds_read_b32 v10, v15 offset:20480\n"
      "ds_read_b32 v11, v15 offset:21504\n"
      "ds_read_b32 v12, v15 offset:22528\n"
      "ds_read_b32 v13, v15 offset:23552\n"
      "s_lshl_b32 s38, s48, 14\n"
      "s_add_u32 s69, s38, 67108864\n"
      "s_lshr_b32 s39, s69, 8\n"
      "s_xor_b32 s39, s39, s50\n"
      "s_and_b32 s39, s39, 8192\n"
      "s_bitset0_b32 s69, 13\n"
      "s_or_b32 s69, s69, s39\n"
      "s_waitcnt lgkmcnt(0)\n"
      "s_lshl_b32 s38, s49, 12\n"
      "s_add_u32 s22, s22, s38\n"
      "s_or_b32 s90, s38, 16384\n"
      "s_cmp_ge_u32 s68, 64\n"
      "s_cselect_b32 s23, s90, s38\n"
      "s_add_u32 s51, s36, 2048\n"
      "s_mov_b32 s44, s51\n"
      "s_min_u32 s45, s36, 2048\n"
      "s_add_u32 s45, s45, 6144\n"
      "s_cmp_lt_u32 s36, 2048\n"
      "s_cselect_b32 s52, s45, s90\n"
      "s_mov_b32 s60, s22\n"
      "s_xor_b32 s61, s60, s51\n"
      "s_xor_b32 s62, s60, s52\n"
      "v_lshrrev_b32 v14, 2, v3\n"
      "s_barrier\n"
      "s_mov_b32 m0, 0\n"
      "s_cmp_lg_u32 s36, 2048\n"
      "s_cbranch_scc1 l015_%=\n"
      "buffer_load_dwordx4 v1, s[40:43], s60 offen sc1 lds\n"
      "buffer_load_dwordx4 v1, s[40:43], s60 offen offset:1024 sc1 lds\n"
      "l015_%=:\n"
      "s_mov_b32 m0, s44\n"
      "s_nop 0\n"
      "buffer_load_dwordx4 v1, s[40:43], s61 offen sc1 lds\n"
      "s_mov_b32 m0, s45\n"
      "s_nop 0\n"
      "buffer_load_dwordx4 v1, s[40:43], s62 offen sc1 lds\n"
      "s_add_u32 m0, s36, 16384\n"
      "s_add_u32 s38, s69, 0\n"
      "buffer_load_dwordx4 v3, s[40:43], s38 offen sc1 lds\n"
      "s_add_u32 m0, s36, 20480\n"
      "s_add_u32 s38, s69, 4096\n"
      "buffer_load_dwordx4 v3, s[40:43], s38 offen sc1 lds\n"
      "s_add_u32 m0, s36, 24576\n"
      "s_add_u32 s38, s69, 524288\n"
      "buffer_load_dwordx4 v3, s[40:43], s38 offen sc1 lds\n"
      "s_add_u32 m0, s36, 28672\n"
      "s_add_u32 s38, s69, 528384\n"
      "buffer_load_dwordx4 v3, s[40:43], s38 offen sc1 lds\n"
      "ds_write_b32 v14, v4 offset:14592\n"
      "ds_write_b128 v2, v[16:19] offset:0\n"
      "ds_write_b128 v2, v[20:23] offset:1024\n"
      "ds_write_b128 v2, v[24:27] offset:2048\n"
      "ds_write_b128 v2, v[28:31] offset:3072\n"
      "ds_write_b128 v2, v[32:35] offset:4096\n"
      "ds_write_b128 v2, v[36:39] offset:5120\n"
      "ds_write_b128 v2, v[40:43] offset:6144\n"
      "ds_write_b128 v2, v[44:47] offset:7168\n"
      "ds_write_b128 v2, v[48:51] offset:8192\n"
      "ds_write_b128 v2, v[52:55] offset:9216\n"
      "ds_write_b128 v2, v[56:59] offset:10240\n"
      "ds_write_b128 v2, v[60:63] offset:11264\n"
      "ds_write_b128 v2, v[64:67] offset:12288\n"
      "ds_write_b128 v2, v[68:71] offset:13312\n"
      "ds_write_b128 v2, v[72:75] offset:14336\n"
      "ds_write_b128 v2, v[76:79] offset:15360\n"
      "ds_write_b128 v2, v[80:83] offset:16384\n"
      "ds_write_b128 v2, v[84:87] offset:17408\n"
      "ds_write_b128 v2, v[88:91] offset:18432\n"
      "ds_write_b128 v2, v[92:95] offset:19456\n"
      "ds_write_b128 v2, v[96:99] offset:20480\n"
      "ds_write_b128 v2, v[100:103] offset:21504\n"
      "ds_write_b128 v2, v[104:107] offset:22528\n"
      "ds_write_b128 v2, v[108:111] offset:23552\n"
      "ds_write_b128 v2, v[112:115] offset:24576\n"
      "ds_write_b128 v2, v[116:119] offset:25600\n"
      "s_mov_b32 s78, 0x3e0293ee\n"
      "s_mov_b32 s79, s78\n"
      "s_lshr_b32 s80, s36, 10\n"
      "s_and_b32 s38, s22, 24576\n"
      "s_xor_b32 s67, s67, s38\n"
      "v_xor_b32 v9, s38, v9\n"
      "s_xor_b32 s46, s38, 16384\n"
      "s_xor_b32 s89, s46, 16384\n"
      "s_lshl_b32 s38, s48, 14\n"
      "s_mul_i32 s39, s49, 4096\n"
      "s_add_u32 s38, s38, s39\n"
      "s_add_u32 s73, s38, 134217728\n"
      "s_lshr_b32 s39, s73, 8\n"
      "s_xor_b32 s39, s39, s50\n"
      "s_and_b32 s39, s39, 8192\n"
      "s_bitset0_b32 s73, 13\n"
      "s_or_b32 s73, s73, s39\n"
      "s_lshl_b32 s38, s49, 12\n"
      "s_lshl_b32 s39, s80, 14\n"
      "s_add_u32 s39, s39, s38\n"
      "s_add_u32 s39, s39, s50\n"
      "s_add_u32 s39, s39, 262144\n"
      "s_and_b32 s38, s80, 1\n"
      "s_lshl_b32 s38, s38, 12\n"
      "s_lshr_b32 s77, s80, 1\n"
      "s_lshl_b32 s77, s77, 14\n"
      "s_add_u32 s77, s77, s38\n"
      "s_lshl_b32 s38, s49, 6\n"
      "s_add_u32 s77, s77, s38\n"
      "s_add_u32 s77, s77, s50\n"
      "s_add_u32 s77, s77, 81920\n"
      "s_bfe_u32 s95, s92, 0x6000a\n"
      "s_add_u32 s94, s50, 49152\n"
      "s_lshl_b32 s38, s95, 7\n"
      "s_add_u32 s38, s38, s94\n"
      "s_lshl_b32 s93, s48, 7\n"
      "s_sub_u32 s38, s38, s93\n"
      "s_lshr_b32 s56, s95, 1\n"
      "s_lshl_b32 s56, s56, 6\n"
      "s_add_u32 s56, s56, s94\n"
      "s_add_u32 s56, s56, 16384\n"
      "s_sub_u32 s56, s56, s93\n"
      "s_cmp_eq_u32 s80, 2\n"
      "s_cselect_b32 s93, s38, s39\n"
      "s_and_b32 s53, s95, 32\n"
      "s_lshr_b32 s53, s53, 2\n"
      "s_xor_b32 s54, s53, 8\n"
      "s_add_u32 s82, s50, 69632\n"
      "s_cmp_eq_u32 s68, 72\n"
      "s_cselect_b32 s81, 1879048192, 0\n"
      "s_mov_b32 s65, 1\n"
      "s_mov_b32 s84, 1985\n"
      "s_mov_b32 s85, 82\n"
      "s_mov_b32 s86, 165\n"
      "s_mov_b32 s87, 2880\n"
      "s_mov_b32 s88, 96\n"
      "s_mov_b32 s47, 24\n"
      "s_sub_u32 s85, 0, s85\n"
      "s_bitcmp1_b32 s68, 4\n"
      "s_cselect_b32 s92, 0, s85\n"
      "s_cmp_eq_u32 s80, 1\n"
      "s_cselect_b32 s92, s86, s92\n"
      "s_add_u32 s92, s92, s84\n"
      "s_sub_u32 s92, s92, 130\n"
      "s_sub_u32 s83, s87, 130\n"
      "s_mov_b32 s66, s88\n"
      "s_sub_u32 s83, s83, s47\n"
      "s_lshr_b32 s85, s47, 1\n"
      "s_sub_u32 s92, s92, s85\n"
      "s_add_u32 s74, s36, 16384\n"
      "v_xor_b32 v13, 49152, v13\n"
      "s_mov_b32 s37, -1\n"
      "s_mov_b32 s99, -1\n"
      "s_mov_b32 s29, 200\n"
      "s_mov_b32 s21, 0\n"
      "s_mov_b32 s58, 0\n"
      "s_mov_b32 s59, 0\n"
      "s_mov_b32 s55, 0\n"
      "s_waitcnt lgkmcnt(0)\n"
      "v_mov_b32 v112, 0x3f803f80\n"
      "v_mov_b32 v113, 0x3f803f80\n"
      "v_mov_b32 v114, 0x3f803f80\n"
      "v_mov_b32 v115, 0x3f803f80\n"
      "v_mov_b32 v14, 0x3f003f00\n"
      "v_mov_b32 v15, 0x3f003f00\n"
      "v_bfe_u32 v108, v1, 4, 4\n"
      "v_mov_b32 v109, 0x3f80\n"
      "v_mov_b32 v110, 0x3f800000\n"
      "v_cmp_eq_u32 vcc, 0, v108\n"
      "v_cmp_eq_u32 s[84:85], 4, v108\n"
      "s_nop 0\n"
      "v_cndmask_b32 v92, 0, v109, vcc\n"
      "v_cndmask_b32 v92, v92, v110, s[84:85]\n"
      "v_cmp_eq_u32 vcc, 1, v108\n"
      "v_cmp_eq_u32 s[84:85], 5, v108\n"
      "s_nop 0\n"
      "v_cndmask_b32 v93, 0, v109, vcc\n"
      "v_cndmask_b32 v93, v93, v110, s[84:85]\n"
      "v_cmp_eq_u32 vcc, 2, v108\n"
      "v_cmp_eq_u32 s[84:85], 6, v108\n"
      "s_nop 0\n"
      "v_cndmask_b32 v94, 0, v109, vcc\n"
      "v_cndmask_b32 v94, v94, v110, s[84:85]\n"
      "v_cmp_eq_u32 vcc, 3, v108\n"
      "v_cmp_eq_u32 s[84:85], 7, v108\n"
      "s_nop 0\n"
      "v_cndmask_b32 v95, 0, v109, vcc\n"
      "v_cndmask_b32 v95, v95, v110, s[84:85]\n"
      "ds_write_b128 v1, v[92:95] offset:13568\n"
      "v_lshl_add_u32 v4, v1, 3, s81\n"
      "ds_read_b128 v[68:71], v2 offset:0\n"
      "ds_read_b128 v[72:75], v2 offset:1024\n"
      "ds_read_b128 v[76:79], v2 offset:2048\n"
      "ds_read_b128 v[80:83], v2 offset:3072\n"
      "ds_read_b128 v[84:87], v2 offset:4096\n"
      "ds_read_b128 v[88:91], v2 offset:5120\n"
      "ds_read_b128 v[32:35], v2 offset:6144\n"
      "ds_read_b128 v[36:39], v2 offset:7168\n"
      "ds_read_b128 v[40:43], v2 offset:8192\n"
      "ds_read_b128 v[44:47], v2 offset:9216\n"
      "ds_read_b128 v[100:103], v2 offset:10240\n"
      "ds_read_b128 v[104:107], v2 offset:11264\n"
      "v_mov_b64 v[52:53], 0\n"
      "v_mov_b64 v[54:55], 0\n"
      "v_mov_b64 v[56:57], 0\n"
      "v_mov_b64 v[58:59], 0\n"
      "v_mov_b64 v[60:61], 0\n"
      "v_mov_b64 v[62:63], 0\n"
      "v_mov_b64 v[64:65], 0\n"
      "v_mov_b64 v[66:67], 0\n"
      "v_mov_b64 v[116:117], -1\n"
      "v_xor_b32 v13, 49152, v13\n"
      "s_mov_b32 s91, 0\n"
      "s_add_u32 s69, s69, 1048576\n"
      "s_lshl_b32 s38, s91, 13\n"
      "s_and_b32 s38, s38, 8192\n"
      "s_xor_b32 s69, s69, s38\n"
      "s_add_u32 s70, s69, 4096\n"
      "s_add_u32 s71, s69, 524288\n"
      "s_add_u32 s57, s71, 4096\n"
      "s_xor_b32 s74, s74, 49152\n"
      "s_mov_b32 s37, 0\n"
      "s_waitcnt vmcnt(0) lgkmcnt(0)\n"
      "s_branch r1b3_%=\n"
      ".p2align 8\n"
      "s_nop 0\n"
      "s_nop 0\n"
      "s_nop 0\n"
      "s_nop 0\n"
      "s_nop 0\n"
      "s_nop 0\n"
      "s_nop 0\n"
      "top1_%=:\n"
      "s_mov_b32 m0, s44\n"
      "s_barrier\n"
      "buffer_load_dwordx4 v1, s[40:43], s61 offen sc1 lds\n"
      "s_mov_b32 m0, s45\n"
      "v_lshrrev_b32 v4, 2, v3\n"
      "ds_read_b32 v4, v4 offset:14592\n"
      "s_nop 15\n"
      "buffer_load_dwordx4 v1, s[40:43], s62 offen sc1 lds\n"
      "s_nop 4\n"
      "s_waitcnt lgkmcnt(0)\n"
      "buffer_store_dwordx4 v[48:51], v3, s[40:43], s72 offen sc1\n"
      "s_waitcnt vmcnt(3)\n"
      "r1b3_%=:\n"
      "s_barrier\n"
      "ds_read_b128 v[16:19], v11 offset:0\n"
      "ds_read_b128 v[20:23], v11 offset:1024\n"
      "s_add_u32 m0, s74, 0\n"
      "s_waitcnt lgkmcnt(1)\n"
      "v_smfmac_f32_16x16x64_bf16 v[52:55], v[16:19], v[68:75], v0\n"
      "ds_read_b128 v[68:71], v2 offset:12288\n"
      "buffer_load_dwordx4 v3, s[40:43], s69 offen sc1 lds\n"
      "v_smfmac_f32_16x16x64_bf16 v[56:59], v[16:19], v[76:83], v0\n"
      "ds_read_b128 v[72:75], v2 offset:13312\n"
      "s_add_u32 m0, s74, 4096\n"
      "v_smfmac_f32_16x16x64_bf16 v[60:63], v[16:19], v[84:91], v0\n"
      "ds_read_b128 v[76:79], v2 offset:14336\n"
      "s_waitcnt lgkmcnt(3)\n"
      "v_smfmac_f32_16x16x64_bf16 v[64:67], v[16:19], v[32:39], v0\n"
      "s_waitcnt vmcnt(3)\n"
      "s_barrier\n"
      "ds_read_b128 v[24:27], v11 offset:2048\n"
      "v_smfmac_f32_16x16x64_bf16 v[52:55], v[20:23], v[40:47], v0\n"
      "ds_read_b128 v[28:31], v11 offset:3072\n"
      "buffer_load_dwordx4 v3, s[40:43], s70 offen sc1 lds\n"
      "v_smfmac_f32_16x16x64_bf16 v[56:59], v[20:23], v[100:107], v0\n"
      "ds_read_b128 v[80:83], v2 offset:15360\n"
      "s_add_u32 m0, s74, 8192\n"
      "v_smfmac_f32_16x16x64_bf16 v[60:63], v[20:23], a[0:7], v0\n"
      "ds_read_b128 v[84:87], v2 offset:16384\n"
      "v_smfmac_f32_16x16x64_bf16 v[64:67], v[20:23], a[8:15], v0\n"
      "ds_read_b128 v[88:91], v2 offset:17408\n"
      "s_waitcnt lgkmcnt(4)\n"
      "v_smfmac_f32_16x16x64_bf16 v[52:55], v[24:27], a[16:23], v0\n"
      "ds_read_b128 v[32:35], v11 offset:4096\n"
      "buffer_load_dwordx4 v3, s[40:43], s71 offen sc1 lds\n"
      "v_smfmac_f32_16x16x64_bf16 v[56:59], v[24:27], a[24:31], v0\n"
      "ds_read_b128 v[36:39], v11 offset:5120\n"
      "s_add_u32 m0, s74, 12288\n"
      "v_add_u32 v110, s36, v13\n"
      "v_smfmac_f32_16x16x64_bf16 v[60:63], v[24:27], a[32:39], v0\n"
      "ds_read_b128 v[92:95], v110 offset:8192\n"
      "v_smfmac_f32_16x16x64_bf16 v[64:67], v[24:27], a[40:47], v0\n"
      "ds_read_b128 v[96:99], v110 offset:12288\n"
      "s_waitcnt lgkmcnt(7)\n"
      "v_smfmac_f32_16x16x64_bf16 v[52:55], v[28:31], a[48:55], v0\n"
      "buffer_load_dwordx4 v3, s[40:43], s57 offen sc1 lds\n"
      "v_smfmac_f32_16x16x64_bf16 v[56:59], v[28:31], v[68:75], v0\n"
      "s_waitcnt lgkmcnt(6)\n"
      "v_smfmac_f32_16x16x64_bf16 v[60:63], v[28:31], v[76:83], v0\n"
      "ds_read_b128 v[68:71], v13 offset:0\n"
      "s_waitcnt lgkmcnt(5)\n"
      "v_smfmac_f32_16x16x64_bf16 v[64:67], v[28:31], v[84:91], v0\n"
      "ds_read_b128 v[72:75], v13 offset:1024\n"
      "s_waitcnt lgkmcnt(2)\n"
      "v_smfmac_f32_16x16x64_bf16 v[52:55], v[32:35], a[56:63], v0\n"
      "s_waitcnt vmcnt(5)\n"
      "s_barrier\n"
      "ds_read_b128 v[40:43], v11 offset:6144\n"
      "v_smfmac_f32_16x16x64_bf16 v[56:59], v[32:35], a[64:71], v0\n"
      "s_memtime s[24:25]\n"
      "ds_read_b128 v[44:47], v11 offset:7168\n"
      "v_mov_b32 v84, 0\n"
      "v_smfmac_f32_16x16x64_bf16 v[60:63], v[32:35], a[72:79], v0\n"
      "ds_read_b128 v[48:51], v11 offset:8192\n"
      "s_memrealtime s[96:97]\n"
      "v_mov_b32 v85, 0\n"
      "v_smfmac_f32_16x16x64_bf16 v[52:55], v[36:39], a[80:87], v0\n"
      "ds_read_b128 v[76:79], v13 offset:2048\n"
      "v_mov_b32 v86, 0\n"
      "v_smfmac_f32_16x16x64_bf16 v[56:59], v[36:39], a[88:95], v0\n"
      "ds_read_b128 v[80:83], v13 offset:3072\n"
      "v_mov_b32 v87, 0\n"
      "v_smfmac_f32_16x16x64_bf16 v[60:63], v[36:39], a[96:103], v0\n"
      "s_waitcnt lgkmcnt(4)\n"
      "v_mov_b32 v88, 0\n"
      "v_smfmac_f32_16x16x64_bf16 v[52:55], v[40:43], a[104:111], v0\n"
      "s_waitcnt lgkmcnt(3)\n"
      "v_mov_b32 v89, 0\n"
      "v_smfmac_f32_16x16x64_bf16 v[52:55], v[44:47], a[112:119], v0\n"
      "s_waitcnt lgkmcnt(2)\n"
      "v_mov_b32 v90, 0\n"
      "v_smfmac_f32_16x16x64_bf16 v[52:55], v[48:51], a[120:127], v0\n"
      "s_waitcnt lgkmcnt(0)\n"
      "v_mov_b32 v91, 0\n"
      "v_smfmac_f32_16x16x64_bf16 v[56:59], v[40:43], a[128:135], v0\n"
      "s_lshl_b32 s96, s96, 6\n"
      "s_or_b32 s96, s96, s37\n"
      "v_mov_b32 v109, s96\n"
      "v_smfmac_f32_16x16x64_bf16 v[56:59], v[44:47], a[136:143], v0\n"
      "v_add_f32 v52, v52, v54\n"
      "v_smfmac_f32_16x16x64_bf16 v[56:59], v[48:51], a[144:151], v0\n"
      "v_add_f32 v53, v53, v55\n"
      "v_smfmac_f32_16x16x64_bf16 v[60:63], v[40:43], a[152:159], v0\n"
      "v_cmp_u_f32 vcc, v52, v53\n"
      "v_smfmac_f32_16x16x64_bf16 v[60:63], v[44:47], a[160:167], v0\n"
      "v_cvt_pk_bf16_f32 v54, v52, v53\n"
      "v_smfmac_f32_16x16x64_bf16 v[60:63], v[48:51], a[168:175], v0\n"
      "v_add_f32 v56, v56, v58\n"
      "v_smfmac_f32_16x16x64_bf16 v[64:67], v[32:35], a[176:183], v0\n"
      "s_cbranch_vccnz r12_%=\n"
      "r2b5_%=:\n"
      "v_add_f32 v57, v57, v59\n"
      "v_smfmac_f32_16x16x64_bf16 v[64:67], v[36:39], a[184:191], v0\n"
      "buffer_store_dword v109, v12, s[40:43], s93 offen sc1\n"
      "v_cvt_pk_bf16_f32 v55, v56, v57\n"
      "v_smfmac_f32_16x16x64_bf16 v[64:67], v[40:43], a[192:199], v0\n"
      "ds_write_b64 v5, v[54:55]\n"
      "v_add_f32 v60, v60, v62\n"
      "v_smfmac_f32_16x16x64_bf16 v[64:67], v[44:47], a[200:207], v0\n"
      "s_add_u32 s91, s37, 1\n"
      "s_and_b32 s38, s91, 3\n"
      "v_add_f32 v61, v61, v63\n"
      "v_smfmac_f32_16x16x64_bf16 v[64:67], v[48:51], a[208:215], v0\n"
      "s_lshl_b32 s38, s38, 15\n"
      "s_add_u32 s60, s38, s22\n"
      "v_cvt_pk_bf16_f32 v62, v60, v61\n"
      "v_smfmac_f32_16x16x64_bf16 v[84:87], v[16:19], a[216:223], v0\n"
      "s_xor_b32 s61, s60, s51\n"
      "s_xor_b32 s62, s60, s52\n"
      "v_smfmac_f32_16x16x64_bf16 v[88:91], v[16:19], a[224:231], v0\n"
      "s_cmp_eq_u32 s37, 63\n"
      "v_add_f32 v64, v64, v66\n"
      "v_add_f32 v65, v65, v67\n"
      "v_cvt_pk_bf16_f32 v63, v64, v65\n"
      "ds_write_b64 v5, v[62:63] offset:128\n"
      "v_smfmac_f32_16x16x64_bf16 v[84:87], v[20:23], a[232:239], v0\n"
      "s_cbranch_scc1 epi8_%=\n"
      "v_smfmac_f32_16x16x64_bf16 v[88:91], v[20:23], a[240:247], v0\n"
      "s_xor_b32 s63, s60, s23\n"
      "s_xor_b32 s64, s63, 0x10000\n"
      "v_smfmac_f32_16x16x64_bf16 v[84:87], v[24:27], a[248:255], v0\n"
      "s_add_u32 s69, s69, 1048576\n"
      "s_lshl_b32 s38, s91, 13\n"
      "v_smfmac_f32_16x16x64_bf16 v[88:91], v[24:27], v[120:127], v0\n"
      "s_waitcnt lgkmcnt(0)\n"
      "s_barrier\n"
      "ds_read_b64_tr_b16 v[52:53], v6 offset:0\n"
      "ds_read_b64_tr_b16 v[54:55], v6 offset:128\n"
      "v_smfmac_f32_16x16x64_bf16 v[84:87], v[28:31], v[128:135], v0\n"
      "ds_read_b64_tr_b16 v[56:57], v6 offset:256\n"
      "ds_read_b64_tr_b16 v[58:59], v6 offset:384\n"
      "ds_read_b64_tr_b16 v[60:61], v6 offset:512\n"
      "v_smfmac_f32_16x16x64_bf16 v[88:91], v[28:31], v[136:143], v0\n"
      "ds_read_b64_tr_b16 v[62:63], v6 offset:640\n"
      "ds_read_b64_tr_b16 v[64:65], v6 offset:768\n"
      "ds_read_b64_tr_b16 v[66:67], v6 offset:896\n"
      "v_smfmac_f32_16x16x64_bf16 v[84:87], v[32:35], v[144:151], v0\n"
      "ds_read_b128 v[16:19], v13 offset:4096\n"
      "s_waitcnt lgkmcnt(7)\n"
      "v_mfma_f32_16x16x32_bf16 v[108:111], v[52:55], v[52:55], 0\n"
      "ds_read_b128 v[24:27], v13 offset:6144\n"
      "v_mfma_f32_16x16x32_bf16 v[100:103], v[68:71], v[52:55], 0\n"
      "ds_read_b128 v[20:23], v13 offset:5120\n"
      "v_mfma_f32_16x16x32_bf16 v[104:107], v[72:75], v[52:55], 0\n"
      "ds_read_b128 v[28:31], v13 offset:7168\n"
      "s_waitcnt lgkmcnt(8)\n"
      "v_mfma_f32_16x16x32_bf16 v[108:111], v[56:59], v[56:59], v[108:111]\n"
      "v_mov_b32 v118, s37\n"
      "v_mfma_f32_16x16x32_bf16 v[100:103], v[76:79], v[56:59], v[100:103]\n"
      "s_waitcnt vmcnt(5)\n"
      "buffer_store_dword v118, v12, s[40:43], s77 offen sc1\n"
      "v_mfma_f32_16x16x32_bf16 v[104:107], v[80:83], v[56:59], v[104:107]\n"
      "s_and_b32 s38, s38, 8192\n"
      "s_xor_b32 s69, s69, s38\n"
      "s_waitcnt lgkmcnt(4)\n"
      "v_mfma_f32_16x16x32_bf16 v[108:111], v[60:63], v[60:63], v[108:111]\n"
      "v_mov_b32 v56, 0\n"
      "v_mov_b32 v57, 0\n"
      "v_mfma_f32_16x16x32_bf16 v[108:111], v[64:67], v[64:67], v[108:111]\n"
      "s_add_u32 s70, s69, 4096\n"
      "s_add_u32 s71, s69, 524288\n"
      "s_waitcnt lgkmcnt(2)\n"
      "v_mfma_f32_16x16x32_bf16 v[100:103], v[16:19], v[60:63], v[100:103]\n"
      "s_add_u32 s57, s71, 4096\n"
      "s_xor_b32 s74, s74, 49152\n"
      "v_mfma_f32_16x16x32_bf16 v[100:103], v[24:27], v[64:67], v[100:103]\n"
      "s_mov_b32 s72, s73\n"
      "s_add_u32 s73, s73, 524288\n"
      "s_waitcnt lgkmcnt(0)\n"
      "v_mfma_f32_16x16x32_bf16 v[104:107], v[20:23], v[60:63], v[104:107]\n"
      "v_max_f32 v68, v110, v111\n"
      "v_mov_b32 v60, 0\n"
      "v_mfma_f32_16x16x32_bf16 v[104:107], v[28:31], v[64:67], v[104:107]\n"
      "v_mov_b32 v61, 0\n"
      "v_max3_f32 v68, v68, v100, v101\n"
      "v_smfmac_f32_16x16x64_bf16 v[88:91], v[32:35], v[152:159], v0\n"
      "ds_read_b128 v[16:19], v2 offset:18432\n"
      "v_max3_f32 v68, v68, v102, v103\n"
      "v_smfmac_f32_16x16x64_bf16 v[84:87], v[36:39], v[160:167], v0\n"
      "v_max3_f32 v68, v68, v104, v105\n"
      "v_max3_f32 v69, v68, v106, v107\n"
      "v_max3_f32 v68, v68, v106, v107\n"
      "v_smfmac_f32_16x16x64_bf16 v[88:91], v[36:39], v[168:175], v0\n"
      "ds_read_b128 v[20:23], v2 offset:19456\n"
      "v_permlane32_swap_b32 v68, v69\n"
      "v_max_f32 v70, v68, v69\n"
      "v_max_f32 v71, v68, v69\n"
      "v_smfmac_f32_16x16x64_bf16 v[84:87], v[40:43], v[176:183], v0\n"
      "ds_read_b128 v[24:27], v2 offset:20480\n"
      "v_permlane16_swap_b32 v70, v71\n"
      "v_max_f32 v68, v70, v71\n"
      "v_mul_f32 v68, s78, v68\n"
      "v_pk_fma_f32 v[100:101], v[100:101], s[78:79], v[68:69] op_sel_hi:[1,1,0] neg_lo:[0,0,1] neg_hi:[0,0,1]\n"
      "v_pk_fma_f32 v[102:103], v[102:103], s[78:79], v[68:69] op_sel_hi:[1,1,0] neg_lo:[0,0,1] neg_hi:[0,0,1]\n"
      "v_pk_fma_f32 v[104:105], v[104:105], s[78:79], v[68:69] op_sel_hi:[1,1,0] neg_lo:[0,0,1] neg_hi:[0,0,1]\n"
      "v_pk_fma_f32 v[106:107], v[106:107], s[78:79], v[68:69] op_sel_hi:[1,1,0] neg_lo:[0,0,1] neg_hi:[0,0,1]\n"
      "v_pk_fma_f32 v[110:111], v[110:111], s[78:79], v[68:69] op_sel_hi:[1,1,0] neg_lo:[0,0,1] neg_hi:[0,0,1]\n"
      "v_smfmac_f32_16x16x64_bf16 v[88:91], v[40:43], v[184:191], v0\n"
      "ds_read_b128 v[28:31], v2 offset:21504\n"
      "v_exp_f32 v100, v100\n"
      "v_smfmac_f32_16x16x64_bf16 v[84:87], v[44:47], v[192:199], v0\n"
      "ds_read_b128 v[32:35], v2 offset:22528\n"
      "v_exp_f32 v101, v101\n"
      "v_smfmac_f32_16x16x64_bf16 v[88:91], v[44:47], v[200:207], v0\n"
      "ds_read_b128 v[36:39], v2 offset:23552\n"
      "v_exp_f32 v102, v102\n"
      "v_smfmac_f32_16x16x64_bf16 v[84:87], v[48:51], v[208:215], v0\n"
      "ds_read_b128 v[40:43], v2 offset:24576\n"
      "v_exp_f32 v103, v103\n"
      "v_smfmac_f32_16x16x64_bf16 v[88:91], v[48:51], v[216:223], v0\n"
      "buffer_load_dword v4, v4, s[40:43], s94 offen sc1\n"
      "ds_read_b128 v[44:47], v2 offset:25600\n"
      "v_exp_f32 v104, v104\n"
      "v_exp_f32 v105, v105\n"
      "v_exp_f32 v106, v106\n"
      "v_exp_f32 v107, v107\n"
      "v_cvt_pk_bf16_f32 v52, v100, v101\n"
      "v_cvt_pk_bf16_f32 v53, v102, v103\n"
      "v_cvt_pk_bf16_f32 v54, v104, v105\n"
      "v_cvt_pk_bf16_f32 v55, v106, v107\n"
      "v_exp_f32 v110, v110\n"
      "v_exp_f32 v111, v111\n"
      "v_mfma_f32_16x16x32_bf16 v[76:79], v[92:95], v[52:55], 0\n"
      "v_cvt_pk_bf16_f32 v58, v110, v111\n"
      "v_cvt_pk_bf16_f32 v59, v110, v111\n"
      "v_mfma_f32_16x16x32_bf16 v[80:83], v[96:99], v[52:55], 0\n"
      "v_cvt_pk_bf16_f32 v48, v84, v85\n"
      "v_cvt_pk_bf16_f32 v49, v86, v87\n"
      "v_mfma_f32_16x16x32_bf16 v[72:75], v[52:55], v[112:115], 0\n"
      "v_cvt_pk_bf16_f32 v50, v88, v89\n"
      "v_cvt_pk_bf16_f32 v51, v90, v91\n"
      "s_nop 0\n"
      "s_nop 0\n"
      "v_mfma_f32_16x16x32_bf16 v[76:79], v[48:51], v[58:61], v[76:79]\n"
      "v_mov_b64 v[62:63], 0\n"
      "v_mfma_f32_16x16x32_bf16 v[80:83], v[48:51], v[56:59], v[80:83]\n"
      "v_mov_b64 v[108:109], 0\n"
      "v_mov_b64 v[110:111], 0\n"
      "s_nop 0\n"
      "v_cvt_pk_bf16_f32 v64, v76, v77\n"
      "v_cvt_pk_bf16_f32 v65, v78, v79\n"
      "s_nop 1\n"
      "v_cvt_pk_bf16_f32 v66, v80, v81\n"
      "v_cvt_pk_bf16_f32 v67, v82, v83\n"
      "ds_write_b128 v3, v[64:67] offset:2048\n"
      "v_mfma_f32_16x16x32_bf16 v[72:75], v[56:59], v[12:15], v[72:75]\n"
      "s_and_b32 s38, s91, 3\n"
      "s_cselect_b32 s38, 0, 8192\n"
      "s_xor_b32 s73, s73, s38\n"
      "s_cmp_lg_u32 s80, 3\n"
      "s_nop 3\n"
      "v_rcp_f32 v66, v72\n"
      "v_rcp_f32 v67, v73\n"
      "s_waitcnt lgkmcnt(0)\n"
      "s_barrier\n"
      "ds_read_b128 v[68:71], v7 offset:2048\n"
      "ds_read_b128 v[72:75], v7 offset:3072\n"
      "ds_read_b128 v[76:79], v7 offset:4096\n"
      "ds_read_b128 v[80:83], v7 offset:5120\n"
      "s_cbranch_scc1 n924_%=\n"
      "ds_read_b128 v[100:103], v2 offset:26624\n"
      "ds_read_b128 v[104:107], v2 offset:27648\n"
      "ds_read_b128 v[84:87], v2 offset:28672\n"
      "ds_read_b128 v[88:91], v2 offset:29696\n"
      "ds_read_b128 v[92:95], v2 offset:30720\n"
      "ds_read_b128 v[96:99], v2 offset:31744\n"
      "ds_read_b128 v[52:55], v2 offset:32768\n"
      "ds_read_b128 v[56:59], v2 offset:33792\n"
      "v_mov_b64 v[116:117], 0\n"
      "v_mov_b64 v[118:119], 0\n"
      "s_waitcnt lgkmcnt(11)\n"
      "v_smfmac_f32_16x16x64_bf16 v[60:63], v[68:71], v[224:231], v0\n"
      "s_waitcnt lgkmcnt(10)\n"
      "v_smfmac_f32_16x16x64_bf16 v[60:63], v[72:75], v[232:239], v0\n"
      "s_waitcnt lgkmcnt(9)\n"
      "v_smfmac_f32_16x16x64_bf16 v[60:63], v[76:79], v[240:247], v0\n"
      "s_waitcnt lgkmcnt(8)\n"
      "v_smfmac_f32_16x16x64_bf16 v[60:63], v[80:83], v[248:255], v0\n"
      "v_smfmac_f32_16x16x64_bf16 v[108:111], v[68:71], v[16:23], v0\n"
      "v_smfmac_f32_16x16x64_bf16 v[108:111], v[72:75], v[24:31], v0\n"
      "v_smfmac_f32_16x16x64_bf16 v[108:111], v[76:79], v[32:39], v0\n"
      "v_mul_f32 v60, v60, v66\n"
      "v_mul_f32 v61, v61, v67\n"
      "v_mul_f32 v62, v62, v66\n"
      "v_mul_f32 v63, v63, v67\n"
      "v_cvt_pk_bf16_f32 v60, v60, v61\n"
      "v_cvt_pk_bf16_f32 v61, v62, v63\n"
      "v_bfi_b32 v60, 1, s37, v60\n"
      "v_bfi_b32 v61, 1, s37, v61\n"
      "buffer_store_dwordx2 v[60:61], v8, s[40:43], s46 offen sc1\n"
      "v_smfmac_f32_16x16x64_bf16 v[108:111], v[80:83], v[40:47], v0\n"
      "s_waitcnt lgkmcnt(0)\n"
      "v_smfmac_f32_16x16x64_bf16 v[116:119], v[68:71], v[100:107], v0\n"
      "s_nop 3\n"
      "v_mul_f32 v108, v108, v66\n"
      "v_mul_f32 v109, v109, v67\n"
      "v_mul_f32 v110, v110, v66\n"
      "v_mul_f32 v111, v111, v67\n"
      "v_cvt_pk_bf16_f32 v108, v108, v109\n"
      "v_cvt_pk_bf16_f32 v109, v110, v111\n"
      "v_bfi_b32 v108, 1, s37, v108\n"
      "v_bfi_b32 v109, 1, s37, v109\n"
      "buffer_store_dwordx2 v[108:109], v8, s[40:43], s89 offen sc1\n"
      "v_smfmac_f32_16x16x64_bf16 v[116:119], v[72:75], v[84:91], v0\n"
      "v_smfmac_f32_16x16x64_bf16 v[116:119], v[76:79], v[92:99], v0\n"
      "v_smfmac_f32_16x16x64_bf16 v[116:119], v[80:83], v[52:59], v0\n"
      "s_nop 7\n"
      "v_pk_mul_f32 v[116:117], v[116:117], v[66:67]\n"
      "v_pk_mul_f32 v[118:119], v[118:119], v[66:67]\n"
      "v_cvt_pk_bf16_f32 v116, v116, v117\n"
      "v_cvt_pk_bf16_f32 v117, v118, v119\n"
      "v_bfi_b32 v116, 1, s37, v116\n"
      "v_bfi_b32 v117, 1, s37, v117\n"
      "buffer_store_dwordx2 v[116:117], v9, s[40:43], 0 offen sc1\n"
      "s_branch ti23_%=\n"
      "n924_%=:\n"
      "s_waitcnt lgkmcnt(3)\n"
      "v_smfmac_f32_16x16x64_bf16 v[60:63], v[68:71], v[224:231], v0\n"
      "s_waitcnt lgkmcnt(2)\n"
      "v_smfmac_f32_16x16x64_bf16 v[60:63], v[72:75], v[232:239], v0\n"
      "s_waitcnt lgkmcnt(1)\n"
      "v_smfmac_f32_16x16x64_bf16 v[60:63], v[76:79], v[240:247], v0\n"
      "s_waitcnt lgkmcnt(0)\n"
      "v_smfmac_f32_16x16x64_bf16 v[60:63], v[80:83], v[248:255], v0\n"
      "v_smfmac_f32_16x16x64_bf16 v[108:111], v[68:71], v[16:23], v0\n"
      "v_smfmac_f32_16x16x64_bf16 v[108:111], v[72:75], v[24:31], v0\n"
      "v_smfmac_f32_16x16x64_bf16 v[108:111], v[76:79], v[32:39], v0\n"
      "v_mul_f32 v60, v60, v66\n"
      "v_mul_f32 v61, v61, v67\n"
      "v_mul_f32 v62, v62, v66\n"
      "v_mul_f32 v63, v63, v67\n"
      "v_cvt_pk_bf16_f32 v60, v60, v61\n"
      "v_cvt_pk_bf16_f32 v61, v62, v63\n"
      "v_bfi_b32 v60, 1, s37, v60\n"
      "v_bfi_b32 v61, 1, s37, v61\n"
      "buffer_store_dwordx2 v[60:61], v8, s[40:43], s46 offen sc1\n"
      "v_smfmac_f32_16x16x64_bf16 v[108:111], v[80:83], v[40:47], v0\n"
      "s_cmp_eq_u32 s68, 72\n"
      "s_nop 6\n"
      "v_pk_mul_f32 v[108:109], v[108:109], v[66:67]\n"
      "v_pk_mul_f32 v[110:111], v[110:111], v[66:67]\n"
      "v_cvt_pk_bf16_f32 v108, v108, v109\n"
      "v_cvt_pk_bf16_f32 v109, v110, v111\n"
      "v_bfi_b32 v108, 1, s37, v108\n"
      "v_bfi_b32 v109, 1, s37, v109\n"
      "buffer_store_dwordx2 v[108:109], v8, s[40:43], s89 offen sc1\n"
      "s_cbranch_scc1 ti23_%=\n"
      "s_cmp_eq_u32 s80, 2\n"
      "s_cbranch_scc1 t218_%=\n"
      "ds_read_b128 v[92:95], v1 offset:13568\n"
      "s_mov_b32 s84, s92\n"
      "s_memtime s[26:27]\n"
      "s_add_u32 s84, s84, s24\n"
      "s_waitcnt lgkmcnt(0)\n"
      "s_sub_i32 s84, s84, s26\n"
      "s_max_i32 s84, s84, 0\n"
      "s_lshr_b32 s84, s84, 3\n"
      "s_min_u32 s84, s84, 56\n"
      "s_lshl_b32 s84, s84, 2\n"
      "s_sub_u32 s84, 236, s84\n"
      "s_getpc_b64 s[86:87]\n"
      "s_add_u32 s86, s86, s84\n"
      "s_addc_u32 s87, s87, 0\n"
      "s_setpc_b64 s[86:87]\n"
      "s_nop 1\n"
      "s_nop 1\n"
      "s_nop 1\n"
      "s_nop 1\n"
      "s_nop 1\n"
      "s_nop 1\n"
      "s_nop 1\n"
      "s_nop 1\n"
      "s_nop 1\n"
      "s_nop 1\n"
      "s_nop 1\n"
      "s_nop 1\n"
      "s_nop 1\n"
      "s_nop 1\n"
      "s_nop 1\n"
      "s_nop 1\n"
      "s_nop 1\n"
      "s_nop 1\n"
      "s_nop 1\n"
      "s_nop 1\n"
      "s_nop 1\n"
      "s_nop 1\n"
      "s_nop 1\n"
      "s_nop 1\n"
      "s_nop 1\n"
      "s_nop 1\n"
      "s_nop 1\n"
      "s_nop 1\n"
      "s_nop 1\n"
      "s_nop 1\n"
      "s_nop 1\n"
      "s_nop 1\n"
      "s_nop 1\n"
      "s_nop 1\n"
      "s_nop 1\n"
      "s_nop 1\n"
      "s_nop 1\n"
      "s_nop 1\n"
      "s_nop 1\n"
      "s_nop 1\n"
      "s_nop 1\n"
      "s_nop 1\n"
      "s_nop 1\n"
      "s_nop 1\n"
      "s_nop 1\n"
      "s_nop 1\n"
      "s_nop 1\n"
      "s_nop 1\n"
      "s_nop 1\n"
      "s_nop 1\n"
      "s_nop 1\n"
      "s_nop 1\n"
      "s_nop 1\n"
      "s_nop 1\n"
      "s_nop 1\n"
      "s_nop 1\n"
      "s_waitcnt vmcnt(2)\n"
      "v_cmp_le_i32 vcc, s37, v4\n"
      "s_nop 0\n"
      "s_or_b32 s85, vcc_hi, s65\n"
      "s_cbranch_scc1 rs25_%=\n"
      "rsb26_%=:\n"
      "buffer_load_dwordx4 v[16:19], v1, s[40:43], s67 offen offset:0 sc1\n"
      "buffer_load_dwordx4 v[20:23], v1, s[40:43], s67 offen offset:1024 sc1\n"
      "buffer_load_dwordx4 v[24:27], v1, s[40:43], s67 offen offset:2048 sc1\n"
      "buffer_load_dwordx4 v[28:31], v1, s[40:43], s67 offen offset:3072 sc1\n"
      "buffer_store_dwordx2 v[116:117], v10, s[40:43], s64 offen sc1\n"
      "ds_read_b128 v[68:71], v2 offset:0\n"
      "ds_read_b128 v[72:75], v2 offset:1024\n"
      "ds_read_b128 v[76:79], v2 offset:2048\n"
      "ds_read_b128 v[80:83], v2 offset:3072\n"
      "ds_read_b128 v[84:87], v2 offset:4096\n"
      "ds_read_b128 v[88:91], v2 offset:5120\n"
      "ds_read_b128 v[32:35], v2 offset:6144\n"
      "ds_read_b128 v[36:39], v2 offset:7168\n"
      "ds_read_b128 v[40:43], v2 offset:8192\n"
      "ds_read_b128 v[44:47], v2 offset:9216\n"
      "ds_read_b128 v[100:103], v2 offset:10240\n"
      "ds_read_b128 v[104:107], v2 offset:11264\n"
      "v_mov_b64 v[52:53], 0\n"
      "v_mov_b64 v[54:55], 0\n"
      "v_mov_b64 v[56:57], 0\n"
      "v_mov_b64 v[58:59], 0\n"
      "v_mov_b64 v[60:61], 0\n"
      "v_mov_b64 v[62:63], 0\n"
      "v_mov_b64 v[64:65], 0\n"
      "v_mov_b64 v[66:67], 0\n"
      "v_xor_b32 v13, 49152, v13\n"
      "s_cmp_lg_u32 s49, 0\n"
      "s_cbranch_scc1 nok28_%=\n"
      "s_lshr_b32 s38, s36, 2\n"
      "v_add_u32 v108, s38, v6\n"
      "ds_read_b64_tr_b16 v[48:49], v108\n"
      "ds_read_b64_tr_b16 v[50:51], v108 offset:128\n"
      "nok28_%=:\n"
      "s_mov_b32 s88, s37\n"
      "s_mov_b32 s37, s91\n"
      "rrb21_%=:\n"
      "v_mov_b32 v111, 0\n"
      "s_waitcnt vmcnt(4)\n"
      "v_mfma_f32_16x16x32_bf16 v[96:99], v[92:95], v[16:19], 0\n"
      "v_bitop3_b32 v109, v16, v17, s88 bitop3:0x7e\n"
      "v_bitop3_b32 v110, v18, v19, s88 bitop3:0x7e\n"
      "v_or3_b32 v111, v111, v109, v110\n"
      "s_waitcnt vmcnt(3)\n"
      "v_mfma_f32_16x16x32_bf16 v[96:99], v[92:95], v[20:23], v[96:99]\n"
      "v_bitop3_b32 v109, v20, v21, s88 bitop3:0x7e\n"
      "v_bitop3_b32 v110, v22, v23, s88 bitop3:0x7e\n"
      "v_or3_b32 v111, v111, v109, v110\n"
      "s_waitcnt vmcnt(2)\n"
      "v_mfma_f32_16x16x32_bf16 v[96:99], v[92:95], v[24:27], v[96:99]\n"
      "v_bitop3_b32 v109, v24, v25, s88 bitop3:0x7e\n"
      "v_bitop3_b32 v110, v26, v27, s88 bitop3:0x7e\n"
      "v_or3_b32 v111, v111, v109, v110\n"
      "s_waitcnt vmcnt(1)\n"
      "v_mfma_f32_16x16x32_bf16 v[96:99], v[92:95], v[28:31], v[96:99]\n"
      "v_bitop3_b32 v109, v28, v29, s88 bitop3:0x7e\n"
      "v_bitop3_b32 v110, v30, v31, s88 bitop3:0x7e\n"
      "v_or3_b32 v111, v111, v109, v110\n"
      "v_and_b32 v111, 1, v111\n"
      "v_cmp_ne_u32 vcc, 0, v111\n"
      "s_nop 1\n"
      "v_add_f32_dpp v96, v96, v96 row_ror:8 row_mask:0xf bank_mask:0xf\n"
      "v_add_f32_dpp v97, v97, v97 row_ror:8 row_mask:0xf bank_mask:0xf\n"
      "v_add_f32_dpp v98, v98, v98 row_ror:8 row_mask:0xf bank_mask:0xf\n"
      "v_add_f32_dpp v99, v99, v99 row_ror:8 row_mask:0xf bank_mask:0xf\n"
      "v_cvt_pk_bf16_f32 v96, v96, v97\n"
      "v_cvt_pk_bf16_f32 v97, v98, v99\n"
      "s_cbranch_vccnz rr20_%=\n"
      "rrc22_%=:\n"
      "buffer_store_dwordx2 v[96:97], v10, s[40:43], s63 offen sc1\n"
      "s_branch top1_%=\n"
      "t218_%=:\n"
      "ds_read_b128 v[68:71], v2 offset:0\n"
      "ds_read_b128 v[72:75], v2 offset:1024\n"
      "ds_read_b128 v[76:79], v2 offset:2048\n"
      "ds_read_b128 v[80:83], v2 offset:3072\n"
      "ds_read_b128 v[84:87], v2 offset:4096\n"
      "ds_read_b128 v[88:91], v2 offset:5120\n"
      "ds_read_b128 v[32:35], v2 offset:6144\n"
      "ds_read_b128 v[36:39], v2 offset:7168\n"
      "ds_read_b128 v[40:43], v2 offset:8192\n"
      "ds_read_b128 v[44:47], v2 offset:9216\n"
      "ds_read_b128 v[100:103], v2 offset:10240\n"
      "ds_read_b128 v[104:107], v2 offset:11264\n"
      "v_mov_b64 v[52:53], 0\n"
      "v_mov_b64 v[54:55], 0\n"
      "v_mov_b64 v[56:57], 0\n"
      "v_mov_b64 v[58:59], 0\n"
      "v_mov_b64 v[60:61], 0\n"
      "v_mov_b64 v[62:63], 0\n"
      "v_mov_b64 v[64:65], 0\n"
      "v_mov_b64 v[66:67], 0\n"
      "v_xor_b32 v13, 49152, v13\n"
      "s_cmp_lg_u32 s49, 0\n"
      "s_cbranch_scc1 nok29_%=\n"
      "s_lshr_b32 s38, s36, 2\n"
      "v_add_u32 v108, s38, v6\n"
      "ds_read_b64_tr_b16 v[48:49], v108\n"
      "ds_read_b64_tr_b16 v[50:51], v108 offset:128\n"
      "nok29_%=:\n"
      "s_nop 15\n"
      "s_nop 15\n"
      "s_nop 7\n"
      "s_waitcnt vmcnt(2)\n"
      "v_readlane_b32 s75, v4, 16\n"
      "s_add_u32 s76, s91, 2\n"
      "s_min_u32 s76, s76, 64\n"
      "s_sub_i32 s84, s96, s48\n"
      "s_mov_b32 s48, s96\n"
      "s_sub_i32 s85, s24, s50\n"
      "s_mov_b32 s50, s24\n"
      "s_mul_i32 s84, s84, 338\n"
      "s_ashr_i32 s84, s84, 10\n"
      "s_sub_i32 s85, s85, s84\n"
      "s_sub_i32 s85, s85, s29\n"
      "s_ashr_i32 s85, s85, 2\n"
      "s_cmp_eq_u32 s59, 1\n"
      "s_cselect_b32 s88, s85, 0\n"
      "v_cmp_le_i32 vcc, s37, v4\n"
      "s_nop 0\n"
      "s_or_b32 s85, vcc_hi, s65\n"
      "s_cbranch_scc1 rs25_%=\n"
      "v_readlane_b32 s96, v4, 0\n"
      "v_and_b32 v108, 63, v4\n"
      "v_cmp_ne_u32 vcc, s37, v108\n"
      "v_subrev_u32 v109, s96, v4\n"
      "s_and_b32 s84, vcc_lo, 0xffff\n"
      "s_cbranch_scc1 rs25_%=\n"
      "v_add_u32_dpp v109, v109, v109 quad_perm:[1,0,3,2] row_mask:0xf bank_mask:0xf\n"
      "s_nop 1\n"
      "v_add_u32_dpp v109, v109, v109 quad_perm:[2,3,0,1] row_mask:0xf bank_mask:0xf\n"
      "s_nop 1\n"
      "v_add_u32_dpp v109, v109, v109 row_half_mirror row_mask:0xf bank_mask:0xf\n"
      "s_nop 1\n"
      "v_readlane_b32 s84, v109, s54\n"
      "v_readlane_b32 s86, v109, s53\n"
      "s_sub_i32 s85, s84, s86\n"
      "s_ashr_i32 s85, s85, 3\n"
      "s_sub_i32 s86, s85, s55\n"
      "s_cmp_eq_u32 s59, 1\n"
      "s_cselect_b32 s86, s86, s58\n"
      "s_add_i32 s58, s58, s86\n"
      "s_ashr_i32 s58, s58, 1\n"
      "s_ashr_i32 s86, s58, 2\n"
      "s_sub_i32 s86, s58, s86\n"
      "s_add_i32 s86, s86, s85\n"
      "s_sub_i32 s84, s86, s66\n"
      "s_max_i32 s84, s84, 0\n"
      "s_add_i32 s86, s86, s66\n"
      "s_min_i32 s86, s86, 0\n"
      "s_sub_i32 s55, s85, s84\n"
      "s_sub_i32 s55, s55, s86\n"
      "s_mov_b32 s59, 1\n"
      "s_mul_i32 s84, s84, 93\n"
      "s_lshr_b32 s84, s84, 8\n"
      "s_add_i32 s29, s29, s88\n"
      "s_mul_i32 s85, s29, 5\n"
      "s_ashr_i32 s85, s85, 5\n"
      "s_add_i32 s84, s84, s85\n"
      "s_add_u32 s84, s84, s83\n"
      "s_memtime s[26:27]\n"
      "s_add_u32 s84, s84, s24\n"
      "s_waitcnt lgkmcnt(0)\n"
      "s_sub_i32 s84, s84, s26\n"
      "s_max_i32 s84, s84, 0\n"
      "s_lshr_b32 s84, s84, 4\n"
      "s_min_u32 s84, s84, 96\n"
      "s_lshl_b32 s84, s84, 2\n"
      "s_sub_u32 s84, 396, s84\n"
      "s_getpc_b64 s[86:87]\n"
      "s_add_u32 s86, s86, s84\n"
      "s_addc_u32 s87, s87, 0\n"
      "s_setpc_b64 s[86:87]\n"
      "s_nop 3\n"
      "s_nop 3\n"
      "s_nop 3\n"
      "s_nop 3\n"
      "s_nop 3\n"
      "s_nop 3\n"
      "s_nop 3\n"
      "s_nop 3\n"
      "s_nop 3\n"
      "s_nop 3\n"
      "s_nop 3\n"
      "s_nop 3\n"
      "s_nop 3\n"
      "s_nop 3\n"
      "s_nop 3\n"
      "s_nop 3\n"
      "s_nop 3\n"
      "s_nop 3\n"
      "s_nop 3\n"
      "s_nop 3\n"
      "s_nop 3\n"
      "s_nop 3\n"
      "s_nop 3\n"
      "s_nop 3\n"
      "s_nop 3\n"
      "s_nop 3\n"
      "s_nop 3\n"
      "s_nop 3\n"
      "s_nop 3\n"
      "s_nop 3\n"
      "s_nop 3\n"
      "s_nop 3\n"
      "s_nop 3\n"
      "s_nop 3\n"
      "s_nop 3\n"
      "s_nop 3\n"
      "s_nop 3\n"
      "s_nop 3\n"
      "s_nop 3\n"
      "s_nop 3\n"
      "s_nop 3\n"
      "s_nop 3\n"
      "s_nop 3\n"
      "s_nop 3\n"
      "s_nop 3\n"
      "s_nop 3\n"
      "s_nop 3\n"
      "s_nop 3\n"
      "s_nop 3\n"
      "s_nop 3\n"
      "s_nop 3\n"
      "s_nop 3\n"
      "s_nop 3\n"
      "s_nop 3\n"
      "s_nop 3\n"
      "s_nop 3\n"
      "s_nop 3\n"
      "s_nop 3\n"
      "s_nop 3\n"
      "s_nop 3\n"
      "s_nop 3\n"
      "s_nop 3\n"
      "s_nop 3\n"
      "s_nop 3\n"
      "s_nop 3\n"
      "s_nop 3\n"
      "s_nop 3\n"
      "s_nop 3\n"
      "s_nop 3\n"
      "s_nop 3\n"
      "s_nop 3\n"
      "s_nop 3\n"
      "s_nop 3\n"
      "s_nop 3\n"
      "s_nop 3\n"
      "s_nop 3\n"
      "s_nop 3\n"
      "s_nop 3\n"
      "s_nop 3\n"
      "s_nop 3\n"
      "s_nop 3\n"
      "s_nop 3\n"
      "s_nop 3\n"
      "s_nop 3\n"
      "s_nop 3\n"
      "s_nop 3\n"
      "s_nop 3\n"
      "s_nop 3\n"
      "s_nop 3\n"
      "s_nop 3\n"
      "s_nop 3\n"
      "s_nop 3\n"
      "s_nop 3\n"
      "s_nop 3\n"
      "s_nop 3\n"
      "s_nop 3\n"
      "rst27_%=:\n"
      "s_mov_b32 s37, s91\n"
      "s_mov_b32 m0, 0\n"
      "s_cmp_lt_i32 s75, s76\n"
      "s_cbranch_scc1 rk6_%=\n"
      "rkb7_%=:\n"
      "buffer_load_dwordx4 v1, s[40:43], s60 offen sc1 lds\n"
      "buffer_load_dwordx4 v1, s[40:43], s60 offen offset:1024 sc1 lds\n"
      "s_nop 9\n"
      "s_branch top1_%=\n"
      "ti23_%=:\n"
      "ds_read_b128 v[68:71], v2 offset:0\n"
      "ds_read_b128 v[72:75], v2 offset:1024\n"
      "ds_read_b128 v[76:79], v2 offset:2048\n"
      "ds_read_b128 v[80:83], v2 offset:3072\n"
      "ds_read_b128 v[84:87], v2 offset:4096\n"
      "ds_read_b128 v[88:91], v2 offset:5120\n"
      "ds_read_b128 v[32:35], v2 offset:6144\n"
      "ds_read_b128 v[36:39], v2 offset:7168\n"
      "ds_read_b128 v[40:43], v2 offset:8192\n"
      "ds_read_b128 v[44:47], v2 offset:9216\n"
      "ds_read_b128 v[100:103], v2 offset:10240\n"
      "ds_read_b128 v[104:107], v2 offset:11264\n"
      "v_mov_b64 v[52:53], 0\n"
      "v_mov_b64 v[54:55], 0\n"
      "v_mov_b64 v[56:57], 0\n"
      "v_mov_b64 v[58:59], 0\n"
      "v_mov_b64 v[60:61], 0\n"
      "v_mov_b64 v[62:63], 0\n"
      "v_mov_b64 v[64:65], 0\n"
      "v_mov_b64 v[66:67], 0\n"
      "v_xor_b32 v13, 49152, v13\n"
      "s_cmp_lg_u32 s49, 0\n"
      "s_cbranch_scc1 nok30_%=\n"
      "s_lshr_b32 s38, s36, 2\n"
      "v_add_u32 v108, s38, v6\n"
      "ds_read_b64_tr_b16 v[48:49], v108\n"
      "ds_read_b64_tr_b16 v[50:51], v108 offset:128\n"
      "nok30_%=:\n"
      "s_mov_b32 s37, s91\n"
      "s_waitcnt vmcnt(0) lgkmcnt(0)\n"
      "s_branch top1_%=\n"
      "r12_%=:\n"
      "v_max_u32 v108, v16, v17\n"
      "v_max3_u32 v108, v108, v18, v19\n"
      "v_max3_u32 v108, v108, v20, v21\n"
      "v_max3_u32 v108, v108, v22, v23\n"
      "v_max3_u32 v108, v108, v24, v25\n"
      "v_max3_u32 v108, v108, v26, v27\n"
      "v_max3_u32 v108, v108, v28, v29\n"
      "v_max3_u32 v108, v108, v30, v31\n"
      "v_max3_u32 v108, v108, v32, v33\n"
      "v_max3_u32 v108, v108, v34, v35\n"
      "v_max3_u32 v108, v108, v36, v37\n"
      "v_max3_u32 v108, v108, v38, v39\n"
      "v_max3_u32 v108, v108, v40, v41\n"
      "v_max3_u32 v108, v108, v42, v43\n"
      "v_max3_u32 v108, v108, v44, v45\n"
      "v_max3_u32 v108, v108, v46, v47\n"
      "v_max3_u32 v108, v108, v48, v49\n"
      "v_max3_u32 v108, v108, v50, v51\n"
      "v_cmp_eq_u32 vcc, -1, v108\n"
      "s_cbranch_vccz r2b5_%=\n"
      "s_add_u32 s21, s21, 1\n"
      "s_cmp_ge_u32 s21, 524288\n"
      "s_cbranch_scc1 r2b5_%=\n"
      "s_cmp_lg_u32 s80, 2\n"
      "s_cbranch_scc1 na33_%=\n"
      "s_cmp_eq_u32 s99, s37\n"
      "s_cbranch_scc1 al34_%=\n"
      "s_mov_b32 s99, s37\n"
      "v_mov_b32 v108, s37\n"
      "buffer_store_dword v108, v12, s[40:43], s56 offen sc1\n"
      "al34_%=:\n"
      "na33_%=:\n"
      "s_waitcnt lgkmcnt(0)\n"
      "s_barrier\n"
      "s_sleep 1\n"
      "s_mov_b32 m0, 0\n"
      "s_cmp_lg_u32 s36, 2048\n"
      "s_cbranch_scc1 l035_%=\n"
      "buffer_load_dwordx4 v1, s[40:43], s60 offen sc1 lds\n"
      "buffer_load_dwordx4 v1, s[40:43], s60 offen offset:1024 sc1 lds\n"
      "l035_%=:\n"
      "s_mov_b32 m0, s44\n"
      "s_nop 0\n"
      "buffer_load_dwordx4 v1, s[40:43], s61 offen sc1 lds\n"
      "s_mov_b32 m0, s45\n"
      "s_nop 0\n"
      "buffer_load_dwordx4 v1, s[40:43], s62 offen sc1 lds\n"
      "ds_read_b128 v[68:71], v2 offset:0\n"
      "ds_read_b128 v[72:75], v2 offset:1024\n"
      "ds_read_b128 v[76:79], v2 offset:2048\n"
      "ds_read_b128 v[80:83], v2 offset:3072\n"
      "ds_read_b128 v[84:87], v2 offset:4096\n"
      "ds_read_b128 v[88:91], v2 offset:5120\n"
      "ds_read_b128 v[32:35], v2 offset:6144\n"
      "ds_read_b128 v[36:39], v2 offset:7168\n"
      "ds_read_b128 v[40:43], v2 offset:8192\n"
      "ds_read_b128 v[44:47], v2 offset:9216\n"
      "ds_read_b128 v[100:103], v2 offset:10240\n"
      "ds_read_b128 v[104:107], v2 offset:11264\n"
      "v_mov_b64 v[52:53], 0\n"
      "v_mov_b64 v[54:55], 0\n"
      "v_mov_b64 v[56:57], 0\n"
      "v_mov_b64 v[58:59], 0\n"
      "v_mov_b64 v[60:61], 0\n"
      "v_mov_b64 v[62:63], 0\n"
      "v_mov_b64 v[64:65], 0\n"
      "v_mov_b64 v[66:67], 0\n"
      "v_lshl_add_u32 v4, v1, 3, s81\n"
      "s_mov_b32 s65, 1\n"
      "s_waitcnt vmcnt(0) lgkmcnt(0)\n"
      "s_branch r1b3_%=\n"
      "rk6_%=:\n"
      "s_cmp_eq_u32 s99, s37\n"
      "s_cbranch_scc1 al36_%=\n"
      "s_mov_b32 s99, s37\n"
      "v_mov_b32 v108, s37\n"
      "buffer_store_dword v108, v12, s[40:43], s56 offen sc1\n"
      "al36_%=:\n"
      "s_sleep 2\n"
      "s_add_u32 s98, s98, 1\n"
      "buffer_load_dword v109, v12, s[40:43], s82 offen sc1\n"
      "s_waitcnt vmcnt(0)\n"
      "v_readfirstlane_b32 s75, v109\n"
      "s_nop 3\n"
      "s_cmp_ge_i32 s75, s76\n"
      "s_cbranch_scc1 rkb7_%=\n"
      "s_cmp_lt_u32 s98, 524288\n"
      "s_cbranch_scc1 rk6_%=\n"
      "s_branch rkb7_%=\n"
      "rr20_%=:\n"
      "s_add_u32 s98, s98, 1\n"
      "s_cmp_ge_u32 s98, 524288\n"
      "s_cbranch_scc1 rrc22_%=\n"
      "s_sleep 1\n"
      "buffer_load_dwordx4 v[16:19], v1, s[40:43], s67 offen offset:0 sc1\n"
      "buffer_load_dwordx4 v[20:23], v1, s[40:43], s67 offen offset:1024 sc1\n"
      "buffer_load_dwordx4 v[24:27], v1, s[40:43], s67 offen offset:2048 sc1\n"
      "buffer_load_dwordx4 v[28:31], v1, s[40:43], s67 offen offset:3072 sc1\n"
      "s_waitcnt vmcnt(0)\n"
      "s_branch rrb21_%=\n"
      "rs25_%=:\n"
      "s_mov_b32 s59, 0\n"
      "s_add_u32 s83, s83, s47\n"
      "s_lshr_b32 s85, s47, 1\n"
      "s_add_u32 s92, s92, s85\n"
      "s_cmp_lg_u32 s65, 0\n"
      "s_mov_b32 s65, 0\n"
      "s_cbranch_scc1 rs041_%=\n"
      "rs137_%=:\n"
      "s_add_u32 s98, s98, 1\n"
      "s_cmp_ge_u32 s98, 524288\n"
      "s_cbranch_scc1 rs440_%=\n"
      "v_lshlrev_b32 v4, 3, v1\n"
      "buffer_load_dword v4, v4, s[40:43], s94 offen sc1\n"
      "s_waitcnt vmcnt(0)\n"
      "rs041_%=:\n"
      "v_readlane_b32 s96, v4, 0\n"
      "v_and_b32 v108, 63, v4\n"
      "v_cmp_ne_u32 vcc, s37, v108\n"
      "v_subrev_u32 v109, s96, v4\n"
      "s_cbranch_vccz rs238_%=\n"
      "s_sleep 6\n"
      "s_branch rs137_%=\n"
      "rs238_%=:\n"
      "s_nop 1\n"
      "v_max_i32_dpp v109, v109, v109 row_ror:8 row_mask:0xf bank_mask:0xf\n"
      "s_nop 1\n"
      "v_max_i32_dpp v109, v109, v109 row_ror:4 row_mask:0xf bank_mask:0xf\n"
      "s_nop 1\n"
      "v_max_i32_dpp v109, v109, v109 row_ror:2 row_mask:0xf bank_mask:0xf\n"
      "s_nop 1\n"
      "v_max_i32_dpp v109, v109, v109 row_ror:1 row_mask:0xf bank_mask:0xf\n"
      "s_nop 0\n"
      "v_readlane_b32 s84, v109, 0\n"
      "v_readlane_b32 s85, v109, 16\n"
      "v_readlane_b32 s86, v109, 32\n"
      "v_readlane_b32 s87, v109, 48\n"
      "s_max_i32 s84, s84, s85\n"
      "s_max_i32 s86, s86, s87\n"
      "s_max_i32 s84, s84, s86\n"
      "s_add_u32 s84, s84, s96\n"
      "s_cmp_eq_u32 s80, 2\n"
      "s_cselect_b32 s85, 2944, 0\n"
      "s_add_u32 s84, s84, s85\n"
      "s_add_u32 s84, s84, 6528\n"
      "s_mov_b32 s85, 0\n"
      "s_cmp_lg_u32 s80, 2\n"
      "s_cbranch_scc1 rs339_%=\n"
      "buffer_load_dword v109, v12, s[40:43], s82 offen sc1\n"
      "s_waitcnt vmcnt(0)\n"
      "v_readfirstlane_b32 s75, v109\n"
      "rs339_%=:\n"
      "s_memrealtime s[96:97]\n"
      "s_waitcnt lgkmcnt(0)\n"
      "s_lshl_b32 s96, s96, 6\n"
      "s_sub_u32 s96, s96, s84\n"
      "s_cmp_ge_i32 s96, 0\n"
      "s_cbranch_scc1 rs440_%=\n"
      "s_add_u32 s85, s85, 1\n"
      "s_cmp_lt_u32 s85, 128\n"
      "s_cbranch_scc1 rs339_%=\n"
      "rs440_%=:\n"
      "s_cmp_lg_u32 s80, 2\n"
      "s_cbranch_scc1 rsb26_%=\n"
      "s_branch rst27_%=\n"
      "epi8_%=:\n"
      "s_waitcnt lgkmcnt(0)\n"
      "s_barrier\n"
      "s_cmp_lg_u32 s49, 0\n"
      "s_cbranch_scc1 nok42_%=\n"
      "s_lshr_b32 s38, s36, 2\n"
      "v_add_u32 v116, s38, v6\n"
      "ds_read_b64_tr_b16 v[48:49], v116\n"
      "ds_read_b64_tr_b16 v[50:51], v116 offset:128\n"
      "s_branch fin43_%=\n"
      "nok42_%=:\n"
      "v_smfmac_f32_16x16x64_bf16 v[88:91], v[20:23], a[240:247], v0\n"
      "v_smfmac_f32_16x16x64_bf16 v[84:87], v[24:27], a[248:255], v0\n"
      "v_smfmac_f32_16x16x64_bf16 v[88:91], v[24:27], v[120:127], v0\n"
      "v_smfmac_f32_16x16x64_bf16 v[84:87], v[28:31], v[128:135], v0\n"
      "v_smfmac_f32_16x16x64_bf16 v[88:91], v[28:31], v[136:143], v0\n"
      "v_smfmac_f32_16x16x64_bf16 v[84:87], v[32:35], v[144:151], v0\n"
      "v_smfmac_f32_16x16x64_bf16 v[88:91], v[32:35], v[152:159], v0\n"
      "v_smfmac_f32_16x16x64_bf16 v[84:87], v[36:39], v[160:167], v0\n"
      "v_smfmac_f32_16x16x64_bf16 v[88:91], v[36:39], v[168:175], v0\n"
      "v_smfmac_f32_16x16x64_bf16 v[84:87], v[40:43], v[176:183], v0\n"
      "v_smfmac_f32_16x16x64_bf16 v[88:91], v[40:43], v[184:191], v0\n"
      "v_smfmac_f32_16x16x64_bf16 v[84:87], v[44:47], v[192:199], v0\n"
      "v_smfmac_f32_16x16x64_bf16 v[88:91], v[44:47], v[200:207], v0\n"
      "v_smfmac_f32_16x16x64_bf16 v[84:87], v[48:51], v[208:215], v0\n"
      "v_smfmac_f32_16x16x64_bf16 v[88:91], v[48:51], v[216:223], v0\n"
      "s_nop 7\n"
      "v_cvt_pk_bf16_f32 v48, v84, v85\n"
      "v_cvt_pk_bf16_f32 v49, v86, v87\n"
      "v_cvt_pk_bf16_f32 v50, v88, v89\n"
      "v_cvt_pk_bf16_f32 v51, v90, v91\n"
      "fin43_%=:\n"
      "v_mov_b32 v116, 63\n"
      "v_mov_b32 v117, 64\n"
      "s_waitcnt vmcnt(5) lgkmcnt(0)\n"
      "buffer_store_dwordx4 v[48:51], v3, s[40:43], s73 offen sc1\n"
      "buffer_store_dword v116, v12, s[40:43], s77 offen sc1\n"
      "s_waitcnt vmcnt(1)\n"
      "buffer_store_dword v117, v12, s[40:43], s77 offen sc1\n"
      "s_waitcnt vmcnt(0)\n"
      ".Lcode_end:\n s_endpgm\n"
      :: "{v0}"(tid) : "memory", "v255", "a255", "s99");
    return;
  }
  const int xj = label - (label > LBL_C0) - (label > LBL_C1), slot = xj * 16 + (rank >> 1);
  const int h = slot / 3, kind = (rank & 1) ? 3 : slot % 3;
  const unsigned wdma = (4 * w + g) * 8192 + h * 256 + (((c >> 1) ^ (g | ((w >> 1) << 2))) * 2 + (c & 1)) * 16;
  const int kr = q | ((g & 1) << 2);
  const unsigned tr0 = (8 * g + q) * 256 + ((2 * w) ^ kr) * 32 + pp * 8, tr1 = (8 * g + q) * 256 + ((2 * w + 1) ^ kr) * 32 + pp * 8;
  if (kind == 0) {
    TAB(TB, 0) = wdma; TAB(TB, 1) = tr0; TAB(TB, 2) = tr1; TAB(TB, 3) = g1; TAB(TB, 4) = l16;
    TAB(TB, 5) = w == 0 ? LNF + l * 128 : (l < 48 ? G2F + ((w - 1) * 48 + l) * 128 : OOB); TAB(TB, 6) = tid == 0 ? KVP + h * 128 : OOB;
    TAB(TB, 7) = hsa;
    TAB(TB, 8) = lo32(p.ws); TAB(TB, 9) = hi32(p.ws); TAB(TB, 10) = lo32(p.wk); TAB(TB, 11) = hi32(p.wk);
    TAB(TB, 12) = lo32(p.wv); TAB(TB, 13) = hi32(p.wv); TAB(TB, 14) = w * 1024; TAB(TB, 15) = h * 16384;
    asm volatile("s_waitcnt lgkmcnt(0)\n"
      "v_lshlrev_b32 v1, 2, v0\n"
      "v_add_u32 v1, 131072, v1\n"
      "ds_read_b32 v2, v1 offset:0\n"
      "ds_read_b32 v3, v1 offset:1024\n"
      "ds_read_b32 v4, v1 offset:2048\n"
      "ds_read_b32 v5, v1 offset:3072\n"
      "ds_read_b32 v6, v1 offset:4096\n"
      "ds_read_b32 v7, v1 offset:5120\n"
      "ds_read_b32 v8, v1 offset:6144\n"
      "ds_read_b32 v24, v1 offset:7168\n"
      "s_waitcnt lgkmcnt(0)\n"
      "ds_read_b32 v16, v1 offset:8192\n"
      "ds_read_b32 v17, v1 offset:9216\n"
      "ds_read_b32 v18, v1 offset:10240\n"
      "ds_read_b32 v19, v1 offset:11264\n"
      "ds_read_b32 v20, v1 offset:12288\n"
      "ds_read_b32 v21, v1 offset:13312\n"
      "ds_read_b32 v22, v1 offset:14336\n"
      "ds_read_b32 v23, v1 offset:15360\n"
      "s_waitcnt lgkmcnt(0)\n"
      "v_readfirstlane_b32 s40, v16\n"
      "v_readfirstlane_b32 s41, v17\n"
      "v_readfirstlane_b32 s48, v18\n"
      "v_readfirstlane_b32 s49, v19\n"
      "v_readfirstlane_b32 s50, v20\n"
      "v_readfirstlane_b32 s51, v21\n"
      "v_readfirstlane_b32 s36, v22\n"
      "v_readfirstlane_b32 s52, v23\n"
      "s_nop 4\n"
      "s_mov_b32 s42, 226492416\n"
      "s_mov_b32 s43, 0x20000\n"
      "s_mov_b32 s46, 4718592\n"
      "s_mov_b32 s47, 0x20000\n"
      "s_mov_b32 s98, 0\n"
      "buffer_load_dword v26, v24, s[40:43], 0 offen sc1\n"
      "s_mov_b32 s44, s48\n"
      "s_mov_b32 s45, s49\n"
      "s_add_u32 m0, s36, 0\n"
      "s_mov_b32 s38, 0\n"
      "buffer_load_dwordx4 v2, s[44:47], s38 offen lds\n"
      "s_add_u32 m0, s36, 4096\n"
      "s_mov_b32 s38, 131072\n"
      "buffer_load_dwordx4 v2, s[44:47], s38 offen lds\n"
      "s_add_u32 m0, s36, 8192\n"
      "s_mov_b32 s38, 262144\n"
      "buffer_load_dwordx4 v2, s[44:47], s38 offen lds\n"
      "s_add_u32 m0, s36, 12288\n"
      "s_mov_b32 s38, 393216\n"
      "buffer_load_dwordx4 v2, s[44:47], s38 offen lds\n"
      "s_add_u32 m0, s36, 16384\n"
      "s_mov_b32 s38, 524288\n"
      "buffer_load_dwordx4 v2, s[44:47], s38 offen lds\n"
      "s_add_u32 m0, s36, 20480\n"
      "s_mov_b32 s38, 655360\n"
      "buffer_load_dwordx4 v2, s[44:47], s38 offen lds\n"
      "s_add_u32 m0, s36, 24576\n"
      "s_mov_b32 s38, 786432\n"
      "buffer_load_dwordx4 v2, s[44:47], s38 offen lds\n"
      "s_add_u32 m0, s36, 28672\n"
      "s_mov_b32 s38, 917504\n"
      "buffer_load_dwordx4 v2, s[44:47], s38 offen lds\n"
      "s_add_u32 m0, s36, 32768\n"
      "s_mov_b32 s38, 1048576\n"
      "buffer_load_dwordx4 v2, s[44:47], s38 offen lds\n"
      "s_add_u32 m0, s36, 36864\n"
      "s_mov_b32 s38, 1179648\n"
      "buffer_load_dwordx4 v2, s[44:47], s38 offen lds\n"
      "s_add_u32 m0, s36, 40960\n"
      "s_mov_b32 s38, 1310720\n"
      "buffer_load_dwordx4 v2, s[44:47], s38 offen lds\n"
      "s_add_u32 m0, s36, 45056\n"
      "s_mov_b32 s38, 1441792\n"
      "buffer_load_dwordx4 v2, s[44:47], s38 offen lds\n"
      "s_add_u32 m0, s36, 49152\n"
      "s_mov_b32 s38, 1572864\n"
      "buffer_load_dwordx4 v2, s[44:47], s38 offen lds\n"
      "s_add_u32 m0, s36, 53248\n"
      "s_mov_b32 s38, 1703936\n"
      "buffer_load_dwordx4 v2, s[44:47], s38 offen lds\n"
      "s_add_u32 m0, s36, 57344\n"
      "s_mov_b32 s38, 1835008\n"
      "buffer_load_dwordx4 v2, s[44:47], s38 offen lds\n"
      "s_add_u32 m0, s36, 61440\n"
      "s_mov_b32 s38, 1966080\n"
      "buffer_load_dwordx4 v2, s[44:47], s38 offen lds\n"
      "s_add_u32 m0, s36, 65536\n"
      "s_mov_b32 s38, 2097152\n"
      "buffer_load_dwordx4 v2, s[44:47], s38 offen lds\n"
      "s_add_u32 m0, s36, 69632\n"
      "s_mov_b32 s38, 2228224\n"
      "buffer_load_dwordx4 v2, s[44:47], s38 offen lds\n"
      "s_add_u32 m0, s36, 73728\n"
      "s_mov_b32 s38, 2359296\n"
      "buffer_load_dwordx4 v2, s[44:47], s38 offen lds\n"
      "s_add_u32 m0, s36, 77824\n"
      "s_mov_b32 s38, 2490368\n"
      "buffer_load_dwordx4 v2, s[44:47], s38 offen lds\n"
      "s_add_u32 m0, s36, 81920\n"
      "s_mov_b32 s38, 2621440\n"
      "buffer_load_dwordx4 v2, s[44:47], s38 offen lds\n"
      "s_add_u32 m0, s36, 86016\n"
      "s_mov_b32 s38, 2752512\n"
      "buffer_load_dwordx4 v2, s[44:47], s38 offen lds\n"
      "s_add_u32 m0, s36, 90112\n"
      "s_mov_b32 s38, 2883584\n"
      "buffer_load_dwordx4 v2, s[44:47], s38 offen lds\n"
      "s_add_u32 m0, s36, 94208\n"
      "s_mov_b32 s38, 3014656\n"
      "buffer_load_dwordx4 v2, s[44:47], s38 offen lds\n"
      "s_add_u32 m0, s36, 98304\n"
      "s_mov_b32 s38, 3145728\n"
      "buffer_load_dwordx4 v2, s[44:47], s38 offen lds\n"
      "s_add_u32 m0, s36, 102400\n"
      "s_mov_b32 s38, 3276800\n"
      "buffer_load_dwordx4 v2, s[44:47], s38 offen lds\n"
      "s_add_u32 m0, s36, 106496\n"
      "s_mov_b32 s38, 3407872\n"
      "buffer_load_dwordx4 v2, s[44:47], s38 offen lds\n"
      "s_add_u32 m0, s36, 110592\n"
      "s_mov_b32 s38, 3538944\n"
      "buffer_load_dwordx4 v2, s[44:47], s38 offen lds\n"
      "s_waitcnt vmcnt(24)\n"
      "s_barrier\n"
      "s_add_u32 m0, s36, 114688\n"
      "s_mov_b32 s38, 3670016\n"
      "buffer_load_dwordx4 v2, s[44:47], s38 offen lds\n"
      "s_add_u32 m0, s36, 118784\n"
      "s_mov_b32 s38, 3801088\n"
      "buffer_load_dwordx4 v2, s[44:47], s38 offen lds\n"
      "s_add_u32 m0, s36, 122880\n"
      "s_mov_b32 s38, 3932160\n"
      "buffer_load_dwordx4 v2, s[44:47], s38 offen lds\n"
      "s_add_u32 m0, s36, 126976\n"
      "s_mov_b32 s38, 4063232\n"
      "buffer_load_dwordx4 v2, s[44:47], s38 offen lds\n"
      "v_readlane_b32 s60, v26, 0\n"
      "v_readlane_b32 s61, v26, 1\n"
      "s_nop 3\n"
      "s_add_u32 s60, s60, 1\n"
      "s_cmp_eq_u32 s60, s61\n"
      "s_addc_u32 s60, s60, 0\n"
      "v_mov_b32 v26, s60\n"
      "s_cmp_eq_u32 s36, 0\n"
      "s_cselect_b32 s61, 0, 1879048192\n"
      "s_cselect_b32 s62, 0, 4\n"
      "s_add_u32 s62, s62, 147456\n"
      "s_mov_b64 exec, 1\n"
      "v_mov_b32 v25, s62\n"
      "ds_write_b32 v25, v26\n"
      "v_add_u32 v25, s61, v24\n"
      "buffer_store_dword v26, v25, s[40:43], 0 offen sc1\n"
      "s_mov_b64 exec, -1\n"
      "v_add_u32 v10, 0, v3\n"
      "v_add_u32 v11, 0, v4\n"
      "ds_read_b64_tr_b16 a[0:1], v10 offset:0\n"
      "ds_read_b64_tr_b16 a[2:3], v10 offset:1024\n"
      "ds_read_b64_tr_b16 a[4:5], v10 offset:8192\n"
      "ds_read_b64_tr_b16 a[6:7], v10 offset:9216\n"
      "ds_read_b64_tr_b16 a[72:73], v11 offset:0\n"
      "ds_read_b64_tr_b16 a[74:75], v11 offset:1024\n"
      "ds_read_b64_tr_b16 a[76:77], v11 offset:8192\n"
      "ds_read_b64_tr_b16 a[78:79], v11 offset:9216\n"
      "s_waitcnt lgkmcnt(0)\n"
      "s_waitcnt vmcnt(25)\n"
      "s_barrier\n"
      "s_add_u32 m0, s36, 0\n"
      "s_mov_b32 s38, 4194304\n"
      "buffer_load_dwordx4 v2, s[44:47], s38 offen lds\n"
      "s_add_u32 m0, s36, 4096\n"
      "s_mov_b32 s38, 4325376\n"
      "buffer_load_dwordx4 v2, s[44:47], s38 offen lds\n"
      "s_add_u32 m0, s36, 8192\n"
      "s_mov_b32 s38, 4456448\n"
      "buffer_load_dwordx4 v2, s[44:47], s38 offen lds\n"
      "s_add_u32 m0, s36, 12288\n"
      "s_mov_b32 s38, 4587520\n"
      "buffer_load_dwordx4 v2, s[44:47], s38 offen lds\n"
      "v_add_u32 v10, 16384, v3\n"
      "v_add_u32 v11, 16384, v4\n"
      "ds_read_b64_tr_b16 a[8:9], v10 offset:0\n"
      "ds_read_b64_tr_b16 a[10:11], v10 offset:1024\n"
      "ds_read_b64_tr_b16 a[12:13], v10 offset:8192\n"
      "ds_read_b64_tr_b16 a[14:15], v10 offset:9216\n"
      "ds_read_b64_tr_b16 a[80:81], v11 offset:0\n"
      "ds_read_b64_tr_b16 a[82:83], v11 offset:1024\n"
      "ds_read_b64_tr_b16 a[84:85], v11 offset:8192\n"
      "ds_read_b64_tr_b16 a[86:87], v11 offset:9216\n"
      "s_waitcnt lgkmcnt(0)\n"
      "s_waitcnt vmcnt(25)\n"
      "s_barrier\n"
      "s_mov_b32 s44, s50\n"
      "s_mov_b32 s45, s51\n"
      "s_add_u32 m0, s36, 16384\n"
      "s_mov_b32 s38, 0\n"
      "buffer_load_dwordx4 v2, s[44:47], s38 offen lds\n"
      "s_add_u32 m0, s36, 20480\n"
      "s_mov_b32 s38, 131072\n"
      "buffer_load_dwordx4 v2, s[44:47], s38 offen lds\n"
      "s_add_u32 m0, s36, 24576\n"
      "s_mov_b32 s38, 262144\n"
      "buffer_load_dwordx4 v2, s[44:47], s38 offen lds\n"
      "s_add_u32 m0, s36, 28672\n"
      "s_mov_b32 s38, 393216\n"
      "buffer_load_dwordx4 v2, s[44:47], s38 offen lds\n"
      "v_add_u32 v10, 32768, v3\n"
      "v_add_u32 v11, 32768, v4\n"
      "ds_read_b64_tr_b16 a[16:17], v10 offset:0\n"
      "ds_read_b64_tr_b16 a[18:19], v10 offset:1024\n"
      "ds_read_b64_tr_b16 a[20:21], v10 offset:8192\n"
      "ds_read_b64_tr_b16 a[22:23], v10 offset:9216\n"
      "ds_read_b64_tr_b16 a[88:89], v11 offset:0\n"
      "ds_read_b64_tr_b16 a[90:91], v11 offset:1024\n"
      "ds_read_b64_tr_b16 a[92:93], v11 offset:8192\n"
      "ds_read_b64_tr_b16 a[94:95], v11 offset:9216\n"
      "s_waitcnt lgkmcnt(0)\n"
      "s_waitcnt vmcnt(25)\n"
      "s_barrier\n"
      "s_add_u32 m0, s36, 32768\n"
      "s_mov_b32 s38, 524288\n"
      "buffer_load_dwordx4 v2, s[44:47], s38 offen lds\n"
      "s_add_u32 m0, s36, 36864\n"
      "s_mov_b32 s38, 655360\n"
      "buffer_load_dwordx4 v2, s[44:47], s38 offen lds\n"
      "s_add_u32 m0, s36, 40960\n"
      "s_mov_b32 s38, 786432\n"
      "buffer_load_dwordx4 v2, s[44:47], s38 offen lds\n"
      "s_add_u32 m0, s36, 45056\n"
      "s_mov_b32 s38, 917504\n"
      "buffer_load_dwordx4 v2, s[44:47], s38 offen lds\n"
      "v_add_u32 v10, 49152, v3\n"
      "v_add_u32 v11, 49152, v4\n"
      "ds_read_b64_tr_b16 a[24:25], v10 offset:0\n"
      "ds_read_b64_tr_b16 a[26:27], v10 offset:1024\n"
      "ds_read_b64_tr_b16 a[28:29], v10 offset:8192\n"
      "ds_read_b64_tr_b16 a[30:31], v10 offset:9216\n"
      "ds_read_b64_tr_b16 a[96:97], v11 offset:0\n"
      "ds_read_b64_tr_b16 a[98:99], v11 offset:1024\n"
      "ds_read_b64_tr_b16 a[100:101], v11 offset:8192\n"
      "ds_read_b64_tr_b16 a[102:103], v11 offset:9216\n"
      "s_waitcnt lgkmcnt(0)\n"
      "s_waitcnt vmcnt(25)\n"
      "s_barrier\n"
      "s_add_u32 m0, s36, 49152\n"
      "s_mov_b32 s38, 1048576\n"
      "buffer_load_dwordx4 v2, s[44:47], s38 offen lds\n"
      "s_add_u32 m0, s36, 53248\n"
      "s_mov_b32 s38, 1179648\n"
      "buffer_load_dwordx4 v2, s[44:47], s38 offen lds\n"
      "s_add_u32 m0, s36, 57344\n"
      "s_mov_b32 s38, 1310720\n"
      "buffer_load_dwordx4 v2, s[44:47], s38 offen lds\n"
      "s_add_u32 m0, s36, 61440\n"
      "s_mov_b32 s38, 1441792\n"
      "buffer_load_dwordx4 v2, s[44:47], s38 offen lds\n"
      "v_add_u32 v10, 65536, v3\n"
      "v_add_u32 v11, 65536, v4\n"
      "ds_read_b64_tr_b16 a[32:33], v10 offset:0\n"
      "ds_read_b64_tr_b16 a[34:35], v10 offset:1024\n"
      "ds_read_b64_tr_b16 a[36:37], v10 offset:8192\n"
      "ds_read_b64_tr_b16 a[38:39], v10 offset:9216\n"
      "ds_read_b64_tr_b16 a[104:105], v11 offset:0\n"
      "ds_read_b64_tr_b16 a[106:107], v11 offset:1024\n"
      "ds_read_b64_tr_b16 a[108:109], v11 offset:8192\n"
      "ds_read_b64_tr_b16 a[110:111], v11 offset:9216\n"
      "s_waitcnt lgkmcnt(0)\n"
      "s_waitcnt vmcnt(25)\n"
      "s_barrier\n"
      "s_add_u32 m0, s36, 65536\n"
      "s_mov_b32 s38, 1572864\n"
      "buffer_load_dwordx4 v2, s[44:47], s38 offen lds\n"
      "s_add_u32 m0, s36, 69632\n"
      "s_mov_b32 s38, 1703936\n"
      "buffer_load_dwordx4 v2, s[44:47], s38 offen lds\n"
      "s_add_u32 m0, s36, 73728\n"
      "s_mov_b32 s38, 1835008\n"
      "buffer_load_dwordx4 v2, s[44:47], s38 offen lds\n"
      "s_add_u32 m0, s36, 77824\n"
      "s_mov_b32 s38, 1966080\n"
      "buffer_load_dwordx4 v2, s[44:47], s38 offen lds\n"
      "v_add_u32 v10, 81920, v3\n"
      "v_add_u32 v11, 81920, v4\n"
      "ds_read_b64_tr_b16 a[40:41], v10 offset:0\n"
      "ds_read_b64_tr_b16 a[42:43], v10 offset:1024\n"
      "ds_read_b64_tr_b16 a[44:45], v10 offset:8192\n"
      "ds_read_b64_tr_b16 a[46:47], v10 offset:9216\n"
      "ds_read_b64_tr_b16 a[112:113], v11 offset:0\n"
      "ds_read_b64_tr_b16 a[114:115], v11 offset:1024\n"
      "ds_read_b64_tr_b16 a[116:117], v11 offset:8192\n"
      "ds_read_b64_tr_b16 a[118:119], v11 offset:9216\n"
      "s_waitcnt lgkmcnt(0)\n"
      "s_waitcnt vmcnt(25)\n"
      "s_barrier\n"
      "s_add_u32 m0, s36, 81920\n"
      "s_mov_b32 s38, 2097152\n"
      "buffer_load_dwordx4 v2, s[44:47], s38 offen lds\n"
      "s_add_u32 m0, s36, 86016\n"
      "s_mov_b32 s38, 2228224\n"
      "buffer_load_dwordx4 v2, s[44:47], s38 offen lds\n"
      "s_add_u32 m0, s36, 90112\n"
      "s_mov_b32 s38, 2359296\n"
      "buffer_load_dwordx4 v2, s[44:47], s38 offen lds\n"
      "s_add_u32 m0, s36, 94208\n"
      "s_mov_b32 s38, 2490368\n"
      "buffer_load_dwordx4 v2, s[44:47], s38 offen lds\n"
      "v_add_u32 v10, 98304, v3\n"
      "v_add_u32 v11, 98304, v4\n"
      "ds_read_b64_tr_b16 a[48:49], v10 offset:0\n"
      "ds_read_b64_tr_b16 a[50:51], v10 offset:1024\n"
      "ds_read_b64_tr_b16 a[52:53], v10 offset:8192\n"
      "ds_read_b64_tr_b16 a[54:55], v10 offset:9216\n"
      "ds_read_b64_tr_b16 a[120:121], v11 offset:0\n"
      "ds_read_b64_tr_b16 a[122:123], v11 offset:1024\n"
      "ds_read_b64_tr_b16 a[124:125], v11 offset:8192\n"
      "ds_read_b64_tr_b16 a[126:127], v11 offset:9216\n"
      "s_waitcnt lgkmcnt(0)\n"
      "s_waitcnt vmcnt(25)\n"
      "s_barrier\n"
      "s_add_u32 m0, s36, 98304\n"
      "s_mov_b32 s38, 2621440\n"
      "buffer_load_dwordx4 v2, s[44:47], s38 offen lds\n"
      "s_add_u32 m0, s36, 102400\n"
      "s_mov_b32 s38, 2752512\n"
      "buffer_load_dwordx4 v2, s[44:47], s38 offen lds\n"
      "s_add_u32 m0, s36, 106496\n"
      "s_mov_b32 s38, 2883584\n"
      "buffer_load_dwordx4 v2, s[44:47], s38 offen lds\n"
      "s_add_u32 m0, s36, 110592\n"
      "s_mov_b32 s38, 3014656\n"
      "buffer_load_dwordx4 v2, s[44:47], s38 offen lds\n"
      "v_add_u32 v10, 114688, v3\n"
      "v_add_u32 v11, 114688, v4\n"
      "ds_read_b64_tr_b16 a[56:57], v10 offset:0\n"
      "ds_read_b64_tr_b16 a[58:59], v10 offset:1024\n"
      "ds_read_b64_tr_b16 a[60:61], v10 offset:8192\n"
      "ds_read_b64_tr_b16 a[62:63], v10 offset:9216\n"
      "ds_read_b64_tr_b16 a[128:129], v11 offset:0\n"
      "ds_read_b64_tr_b16 a[130:131], v11 offset:1024\n"
      "ds_read_b64_tr_b16 a[132:133], v11 offset:8192\n"
      "ds_read_b64_tr_b16 a[134:135], v11 offset:9216\n"
      "s_waitcnt lgkmcnt(0)\n"
      "s_waitcnt vmcnt(24)\n"
      "s_barrier\n"
      "s_add_u32 m0, s36, 114688\n"
      "s_mov_b32 s38, 3145728\n"
      "buffer_load_dwordx4 v2, s[44:47], s38 offen lds\n"
      "s_add_u32 m0, s36, 118784\n"
      "s_mov_b32 s38, 3276800\n"
      "buffer_load_dwordx4 v2, s[44:47], s38 offen lds\n"
      "s_add_u32 m0, s36, 122880\n"
      "s_mov_b32 s38, 3407872\n"
      "buffer_load_dwordx4 v2, s[44:47], s38 offen lds\n"
      "s_add_u32 m0, s36, 126976\n"
      "s_mov_b32 s38, 3538944\n"
      "buffer_load_dwordx4 v2, s[44:47], s38 offen lds\n"
      "v_add_u32 v10, 0, v3\n"
      "v_add_u32 v11, 0, v4\n"
      "ds_read_b64_tr_b16 a[64:65], v10 offset:0\n"
      "ds_read_b64_tr_b16 a[66:67], v10 offset:1024\n"
      "ds_read_b64_tr_b16 a[68:69], v10 offset:8192\n"
      "ds_read_b64_tr_b16 a[70:71], v10 offset:9216\n"
      "ds_read_b64_tr_b16 a[136:137], v11 offset:0\n"
      "ds_read_b64_tr_b16 a[138:139], v11 offset:1024\n"
      "ds_read_b64_tr_b16 a[140:141], v11 offset:8192\n"
      "ds_read_b64_tr_b16 a[142:143], v11 offset:9216\n"
      "s_waitcnt lgkmcnt(0)\n"
      "s_waitcnt vmcnt(24)\n"
      "s_barrier\n"
      "s_add_u32 m0, s36, 0\n"
      "s_mov_b32 s38, 3670016\n"
      "buffer_load_dwordx4 v2, s[44:47], s38 offen lds\n"
      "s_add_u32 m0, s36, 4096\n"
      "s_mov_b32 s38, 3801088\n"
      "buffer_load_dwordx4 v2, s[44:47], s38 offen lds\n"
      "s_add_u32 m0, s36, 8192\n"
      "s_mov_b32 s38, 3932160\n"
      "buffer_load_dwordx4 v2, s[44:47], s38 offen lds\n"
      "s_add_u32 m0, s36, 12288\n"
      "s_mov_b32 s38, 4063232\n"
      "buffer_load_dwordx4 v2, s[44:47], s38 offen lds\n"
      "v_add_u32 v10, 16384, v3\n"
      "v_add_u32 v11, 16384, v4\n"
      "ds_read_b64_tr_b16 a[144:145], v10 offset:0\n"
      "ds_read_b64_tr_b16 a[146:147], v10 offset:1024\n"
      "ds_read_b64_tr_b16 a[148:149], v10 offset:8192\n"
      "ds_read_b64_tr_b16 a[150:151], v10 offset:9216\n"
      "ds_read_b64_tr_b16 a[216:217], v11 offset:0\n"
      "ds_read_b64_tr_b16 a[218:219], v11 offset:1024\n"
      "ds_read_b64_tr_b16 a[220:221], v11 offset:8192\n"
      "ds_read_b64_tr_b16 a[222:223], v11 offset:9216\n"
      "s_waitcnt lgkmcnt(0)\n"
      "s_waitcnt vmcnt(24)\n"
      "s_barrier\n"
      "s_add_u32 m0, s36, 16384\n"
      "s_mov_b32 s38, 4194304\n"
      "buffer_load_dwordx4 v2, s[44:47], s38 offen lds\n"
      "s_add_u32 m0, s36, 20480\n"
      "s_mov_b32 s38, 4325376\n"
      "buffer_load_dwordx4 v2, s[44:47], s38 offen lds\n"
      "s_add_u32 m0, s36, 24576\n"
      "s_mov_b32 s38, 4456448\n"
      "buffer_load_dwordx4 v2, s[44:47], s38 offen lds\n"
      "s_add_u32 m0, s36, 28672\n"
      "s_mov_b32 s38, 4587520\n"
      "buffer_load_dwordx4 v2, s[44:47], s38 offen lds\n"
      "v_mov_b32 v25, 147456\n"
      "ds_read_b32 v25, v25\n"
      "buffer_load_dwordx2 v[26:27], v24, s[40:43], 0 offen sc1\n"
      "s_waitcnt lgkmcnt(0)\n"
      "v_readfirstlane_b32 s60, v25\n"
      "v_add_u32 v10, 32768, v3\n"
      "v_add_u32 v11, 32768, v4\n"
      "ds_read_b64_tr_b16 a[152:153], v10 offset:0\n"
      "ds_read_b64_tr_b16 a[154:155], v10 offset:1024\n"
      "ds_read_b64_tr_b16 a[156:157], v10 offset:8192\n"
      "ds_read_b64_tr_b16 a[158:159], v10 offset:9216\n"
      "ds_read_b64_tr_b16 a[224:225], v11 offset:0\n"
      "ds_read_b64_tr_b16 a[226:227], v11 offset:1024\n"
      "ds_read_b64_tr_b16 a[228:229], v11 offset:8192\n"
      "ds_read_b64_tr_b16 a[230:231], v11 offset:9216\n"
      "s_waitcnt lgkmcnt(0)\n"
      "s_waitcnt vmcnt(25)\n"
      "s_barrier\n"
      "v_add_u32 v10, 49152, v3\n"
      "v_add_u32 v11, 49152, v4\n"
      "ds_read_b64_tr_b16 a[160:161], v10 offset:0\n"
      "ds_read_b64_tr_b16 a[162:163], v10 offset:1024\n"
      "ds_read_b64_tr_b16 a[164:165], v10 offset:8192\n"
      "ds_read_b64_tr_b16 a[166:167], v10 offset:9216\n"
      "ds_read_b64_tr_b16 a[232:233], v11 offset:0\n"
      "ds_read_b64_tr_b16 a[234:235], v11 offset:1024\n"
      "ds_read_b64_tr_b16 a[236:237], v11 offset:8192\n"
      "ds_read_b64_tr_b16 a[238:239], v11 offset:9216\n"
      "s_waitcnt lgkmcnt(0)\n"
      "s_waitcnt vmcnt(21)\n"
      "s_barrier\n"
      "v_add_u32 v10, 65536, v3\n"
      "v_add_u32 v11, 65536, v4\n"
      "ds_read_b64_tr_b16 a[168:169], v10 offset:0\n"
      "ds_read_b64_tr_b16 a[170:171], v10 offset:1024\n"
      "ds_read_b64_tr_b16 a[172:173], v10 offset:8192\n"
      "ds_read_b64_tr_b16 a[174:175], v10 offset:9216\n"
      "ds_read_b64_tr_b16 a[240:241], v11 offset:0\n"
      "ds_read_b64_tr_b16 a[242:243], v11 offset:1024\n"
      "ds_read_b64_tr_b16 a[244:245], v11 offset:8192\n"
      "ds_read_b64_tr_b16 a[246:247], v11 offset:9216\n"
      "s_waitcnt lgkmcnt(0)\n"
      "s_waitcnt vmcnt(17)\n"
      "s_barrier\n"
      "v_add_u32 v10, 81920, v3\n"
      "v_add_u32 v11, 81920, v4\n"
      "ds_read_b64_tr_b16 a[176:177], v10 offset:0\n"
      "ds_read_b64_tr_b16 a[178:179], v10 offset:1024\n"
      "ds_read_b64_tr_b16 a[180:181], v10 offset:8192\n"
      "ds_read_b64_tr_b16 a[182:183], v10 offset:9216\n"
      "ds_read_b64_tr_b16 a[248:249], v11 offset:0\n"
      "ds_read_b64_tr_b16 a[250:251], v11 offset:1024\n"
      "ds_read_b64_tr_b16 a[252:253], v11 offset:8192\n"
      "ds_read_b64_tr_b16 a[254:255], v11 offset:9216\n"
      "s_waitcnt lgkmcnt(0)\n"
      "s_waitcnt vmcnt(13)\n"
      "s_barrier\n"
      "v_add_u32 v10, 98304, v3\n"
      "v_add_u32 v11, 98304, v4\n"
      "ds_read_b64_tr_b16 a[184:185], v10 offset:0\n"
      "ds_read_b64_tr_b16 a[186:187], v10 offset:1024\n"
      "ds_read_b64_tr_b16 a[188:189], v10 offset:8192\n"
      "ds_read_b64_tr_b16 a[190:191], v10 offset:9216\n"
      "ds_read_b64_tr_b16 v[224:225], v11 offset:0\n"
      "ds_read_b64_tr_b16 v[226:227], v11 offset:1024\n"
      "ds_read_b64_tr_b16 v[228:229], v11 offset:8192\n"
      "ds_read_b64_tr_b16 v[230:231], v11 offset:9216\n"
      "s_waitcnt lgkmcnt(0)\n"
      "s_waitcnt vmcnt(9)\n"
      "s_barrier\n"
      "v_add_u32 v10, 114688, v3\n"
      "v_add_u32 v11, 114688, v4\n"
      "ds_read_b64_tr_b16 a[192:193], v10 offset:0\n"
      "ds_read_b64_tr_b16 a[194:195], v10 offset:1024\n"
      "ds_read_b64_tr_b16 a[196:197], v10 offset:8192\n"
      "ds_read_b64_tr_b16 a[198:199], v10 offset:9216\n"
      "ds_read_b64_tr_b16 v[232:233], v11 offset:0\n"
      "ds_read_b64_tr_b16 v[234:235], v11 offset:1024\n"
      "ds_read_b64_tr_b16 v[236:237], v11 offset:8192\n"
      "ds_read_b64_tr_b16 v[238:239], v11 offset:9216\n"
      "s_waitcnt lgkmcnt(0)\n"
      "s_waitcnt vmcnt(5)\n"
      "s_barrier\n"
      "v_add_u32 v10, 0, v3\n"
      "v_add_u32 v11, 0, v4\n"
      "ds_read_b64_tr_b16 a[200:201], v10 offset:0\n"
      "ds_read_b64_tr_b16 a[202:203], v10 offset:1024\n"
      "ds_read_b64_tr_b16 a[204:205], v10 offset:8192\n"
      "ds_read_b64_tr_b16 a[206:207], v10 offset:9216\n"
      "ds_read_b64_tr_b16 v[240:241], v11 offset:0\n"
      "ds_read_b64_tr_b16 v[242:243], v11 offset:1024\n"
      "ds_read_b64_tr_b16 v[244:245], v11 offset:8192\n"
      "ds_read_b64_tr_b16 v[246:247], v11 offset:9216\n"
      "s_waitcnt lgkmcnt(0)\n"
      "s_waitcnt vmcnt(1)\n"
      "s_barrier\n"
      "v_add_u32 v10, 16384, v3\n"
      "v_add_u32 v11, 16384, v4\n"
      "ds_read_b64_tr_b16 a[208:209], v10 offset:0\n"
      "ds_read_b64_tr_b16 a[210:211], v10 offset:1024\n"
      "ds_read_b64_tr_b16 a[212:213], v10 offset:8192\n"
      "ds_read_b64_tr_b16 a[214:215], v10 offset:9216\n"
      "ds_read_b64_tr_b16 v[248:249], v11 offset:0\n"
      "ds_read_b64_tr_b16 v[250:251], v11 offset:1024\n"
      "ds_read_b64_tr_b16 v[252:253], v11 offset:8192\n"
      "ds_read_b64_tr_b16 v[254:255], v11 offset:9216\n"
      "s_waitcnt lgkmcnt(0)\n"
      "s_waitcnt vmcnt(0)\n"
      "v_readlane_b32 s61, v26, 1\n"
      "v_readlane_b32 s62, v27, 1\n"
      "s_nop 3\n"
      "s_cmp_eq_u32 s60, s61\n"
      "s_cselect_b32 s63, 1, 0\n"
      "s_cselect_b32 s61, s36, 1\n"
      "s_cmp_eq_u32 s61, 0\n"
      "s_cselect_b32 s61, 4, 1879048192\n"
      "s_mov_b64 exec, 1\n"
      "v_mov_b32 v26, s62\n"
      "v_add_u32 v25, s61, v24\n"
      "buffer_store_dword v26, v25, s[40:43], 0 offen sc1\n"
      "s_mov_b64 exec, -1\n"
      "s_cmp_eq_u32 s63, 1\n"
      "s_cbranch_scc1 hd45_%=\n"
      "hs44_%=:\n"
      "s_sleep 2\n"
      "buffer_load_dwordx2 v[26:27], v24, s[40:43], 0 offen sc1\n"
      "s_waitcnt vmcnt(0)\n"
      "v_readlane_b32 s61, v26, 1\n"
      "v_readlane_b32 s62, v27, 1\n"
      "s_nop 3\n"
      "s_cmp_eq_u32 s60, s61\n"
      "s_cselect_b32 s63, 1, 0\n"
      "s_cselect_b32 s61, s36, 1\n"
      "s_cmp_eq_u32 s61, 0\n"
      "s_cselect_b32 s61, 4, 1879048192\n"
      "s_mov_b64 exec, 1\n"
      "v_mov_b32 v26, s62\n"
      "v_add_u32 v25, s61, v24\n"
      "buffer_store_dword v26, v25, s[40:43], 0 offen sc1\n"
      "s_mov_b64 exec, -1\n"
      "s_add_u32 s98, s98, 1\n"
      "s_cmp_ge_u32 s98, 524288\n"
      "s_cselect_b32 s63, 1, s63\n"
      "s_cmp_eq_u32 s63, 1\n"
      "s_cbranch_scc0 hs44_%=\n"
      "hd45_%=:\n"
      "s_cmp_eq_u32 s36, 3072\n"
      "s_cbranch_scc1 lw46_%=\n"
      "s_mov_b32 s39, s98\n"
      "wf47_%=:\n"
      "buffer_load_dword v12, v7, s[40:43], 0 offen sc1\n"
      "s_waitcnt vmcnt(0)\n"
      "v_cmp_gt_i32 vcc, 0, v12\n"
      "s_cbranch_vccz wd48_%=\n"
      "s_sleep 4\n"
      "s_add_u32 s39, s39, 1\n"
      "s_cmp_lt_u32 s39, 524288\n"
      "s_cbranch_scc1 wf47_%=\n"
      "s_mov_b32 s98, 524288\n"
      "wd48_%=:\n"
      "lw46_%=:\n"
      "v_mov_b32 v13, 40960\n"
      "s_mov_b32 s39, s98\n"
      "s_mov_b64 exec, 1\n"
      "wf49_%=:\n"
      "buffer_load_dword v12, v13, s[40:43], 0 offen sc1\n"
      "s_waitcnt vmcnt(0)\n"
      "v_cmp_gt_i32 vcc, 0, v12\n"
      "s_cbranch_vccz wd50_%=\n"
      "s_sleep 4\n"
      "s_add_u32 s39, s39, 1\n"
      "s_cmp_lt_u32 s39, 524288\n"
      "s_cbranch_scc1 wf49_%=\n"
      "s_mov_b32 s98, 524288\n"
      "wd50_%=:\n"
      "s_mov_b64 exec, -1\n"
      "v_readfirstlane_b32 s38, v12\n"
      "s_nop 3\n"
      "s_and_b32 s90, s38, 8192\n"
      "s_xor_b32 s91, s90, 8192\n"
      "v_add_u32 v8, s90, v8\n"
      "s_barrier\n"
      "s_mov_b32 s53, 2097152\n"
      "s_mov_b32 s54, s36\n"
      "s_mov_b32 s55, 0\n"
      "s_add_u32 s56, s52, 67108864\n"
      "s_mov_b32 s37, 0\n"
      "s_lshr_b32 s39, s56, 8\n"
      "s_xor_b32 s39, s39, s90\n"
      "s_and_b32 s39, s39, 8192\n"
      "s_bitset0_b32 s56, 13\n"
      "s_or_b32 s56, s56, s39\n"
      "s_add_u32 m0, s54, 0\n"
      "s_add_u32 s38, s53, 0\n"
      "buffer_load_dwordx4 v5, s[40:43], s38 offen sc1 lds\n"
      "s_add_u32 m0, s54, 4096\n"
      "s_add_u32 s38, s53, 4096\n"
      "buffer_load_dwordx4 v5, s[40:43], s38 offen sc1 lds\n"
      "s_add_u32 m0, s54, 8192\n"
      "s_add_u32 s38, s53, 8192\n"
      "buffer_load_dwordx4 v5, s[40:43], s38 offen sc1 lds\n"
      "s_add_u32 m0, s54, 12288\n"
      "s_add_u32 s38, s53, 12288\n"
      "buffer_load_dwordx4 v5, s[40:43], s38 offen sc1 lds\n"
      "s_add_u32 m0, s54, 16384\n"
      "s_add_u32 s38, s53, 16384\n"
      "buffer_load_dwordx4 v5, s[40:43], s38 offen sc1 lds\n"
      "s_add_u32 m0, s54, 20480\n"
      "s_add_u32 s38, s53, 20480\n"
      "buffer_load_dwordx4 v5, s[40:43], s38 offen sc1 lds\n"
      "s_add_u32 m0, s54, 24576\n"
      "s_add_u32 s38, s53, 24576\n"
      "buffer_load_dwordx4 v5, s[40:43], s38 offen sc1 lds\n"
      "s_add_u32 m0, s54, 28672\n"
      "s_add_u32 s38, s53, 28672\n"
      "buffer_load_dwordx4 v5, s[40:43], s38 offen sc1 lds\n"
      "s_add_u32 m0, s54, 32768\n"
      "s_add_u32 s38, s53, 32768\n"
      "buffer_load_dwordx4 v5, s[40:43], s38 offen sc1 lds\n"
      "s_add_u32 s53, s53, 36864\n"
      "s_add_u32 s54, s54, 36864\n"
      "s_cmp_ge_u32 s54, 110592\n"
      "s_cselect_b32 s38, 110592, 0\n"
      "s_sub_u32 s54, s54, s38\n"
      "s_add_u32 m0, s54, 0\n"
      "s_add_u32 s38, s53, 0\n"
      "buffer_load_dwordx4 v5, s[40:43], s38 offen sc1 lds\n"
      "s_add_u32 m0, s54, 4096\n"
      "s_add_u32 s38, s53, 4096\n"
      "buffer_load_dwordx4 v5, s[40:43], s38 offen sc1 lds\n"
      "s_add_u32 m0, s54, 8192\n"
      "s_add_u32 s38, s53, 8192\n"
      "buffer_load_dwordx4 v5, s[40:43], s38 offen sc1 lds\n"
      "s_add_u32 m0, s54, 12288\n"
      "s_add_u32 s38, s53, 12288\n"
      "buffer_load_dwordx4 v5, s[40:43], s38 offen sc1 lds\n"
      "s_add_u32 m0, s54, 16384\n"
      "s_add_u32 s38, s53, 16384\n"
      "buffer_load_dwordx4 v5, s[40:43], s38 offen sc1 lds\n"
      "s_add_u32 m0, s54, 20480\n"
      "s_add_u32 s38, s53, 20480\n"
      "buffer_load_dwordx4 v5, s[40:43], s38 offen sc1 lds\n"
      "s_add_u32 m0, s54, 24576\n"
      "s_add_u32 s38, s53, 24576\n"
      "buffer_load_dwordx4 v5, s[40:43], s38 offen sc1 lds\n"
      "s_add_u32 m0, s54, 28672\n"
      "s_add_u32 s38, s53, 28672\n"
      "buffer_load_dwordx4 v5, s[40:43], s38 offen sc1 lds\n"
      "s_add_u32 m0, s54, 32768\n"
      "s_add_u32 s38, s53, 32768\n"
      "buffer_load_dwordx4 v5, s[40:43], s38 offen sc1 lds\n"
      "s_add_u32 s53, s53, 36864\n"
      "s_add_u32 s54, s54, 36864\n"
      "s_cmp_ge_u32 s54, 110592\n"
      "s_cselect_b32 s38, 110592, 0\n"
      "s_sub_u32 s54, s54, s38\n"
      "s_waitcnt vmcnt(9)\n"
      "s_add_u32 s57, s56, s36\n"
      "s_add_u32 s58, s56, 524288\n"
      "s_add_u32 s59, s58, 4096\n"
      "kvtop51_%=:\n"
      "s_barrier\n"
      "v_add_u32 v9, s55, v6\n"
      "ds_read_b128 v[64:67], v9 offset:0\n"
      "ds_read_b128 v[68:71], v9 offset:1024\n"
      "ds_read_b128 v[72:75], v9 offset:2048\n"
      "ds_read_b128 v[76:79], v9 offset:3072\n"
      "ds_read_b128 v[80:83], v9 offset:4096\n"
      "ds_read_b128 v[84:87], v9 offset:5120\n"
      "s_waitcnt lgkmcnt(4)\n"
      "v_mfma_f32_16x16x32_bf16 v[32:35], a[0:3], v[64:67], 0\n"
      "v_mfma_f32_16x16x32_bf16 v[48:51], v[64:67], a[144:147], 0\n"
      "v_mfma_f32_16x16x32_bf16 v[36:39], a[0:3], v[68:71], 0\n"
      "v_mfma_f32_16x16x32_bf16 v[52:55], v[68:71], a[144:147], 0\n"
      "buffer_store_dwordx4 v[96:99], v5, s[40:43], s57 offen offset:0 sc1\n"
      "v_mfma_f32_16x16x32_bf16 v[40:43], a[72:75], v[64:67], 0\n"
      "v_mfma_f32_16x16x32_bf16 v[56:59], v[64:67], a[216:219], 0\n"
      "v_mfma_f32_16x16x32_bf16 v[44:47], a[72:75], v[68:71], 0\n"
      "v_mfma_f32_16x16x32_bf16 v[60:63], v[68:71], a[216:219], 0\n"
      "ds_read_b128 v[64:67], v9 offset:6144\n"
      "ds_read_b128 v[68:71], v9 offset:7168\n"
      "s_waitcnt lgkmcnt(4)\n"
      "v_mfma_f32_16x16x32_bf16 v[32:35], a[4:7], v[72:75], v[32:35]\n"
      "v_mfma_f32_16x16x32_bf16 v[48:51], v[72:75], a[148:151], v[48:51]\n"
      "v_mfma_f32_16x16x32_bf16 v[36:39], a[4:7], v[76:79], v[36:39]\n"
      "v_mfma_f32_16x16x32_bf16 v[52:55], v[76:79], a[148:151], v[52:55]\n"
      "buffer_store_dwordx4 v[100:103], v5, s[40:43], s57 offen offset:1024 sc1\n"
      "v_mfma_f32_16x16x32_bf16 v[40:43], a[76:79], v[72:75], v[40:43]\n"
      "v_mfma_f32_16x16x32_bf16 v[56:59], v[72:75], a[220:223], v[56:59]\n"
      "v_mfma_f32_16x16x32_bf16 v[44:47], a[76:79], v[76:79], v[44:47]\n"
      "v_mfma_f32_16x16x32_bf16 v[60:63], v[76:79], a[220:223], v[60:63]\n"
      "ds_read_b128 v[72:75], v9 offset:8192\n"
      "ds_read_b128 v[76:79], v9 offset:9216\n"
      "s_waitcnt lgkmcnt(4)\n"
      "v_mfma_f32_16x16x32_bf16 v[32:35], a[8:11], v[80:83], v[32:35]\n"
      "v_mfma_f32_16x16x32_bf16 v[48:51], v[80:83], a[152:155], v[48:51]\n"
      "v_mfma_f32_16x16x32_bf16 v[36:39], a[8:11], v[84:87], v[36:39]\n"
      "v_mfma_f32_16x16x32_bf16 v[52:55], v[84:87], a[152:155], v[52:55]\n"
      "buffer_store_dwordx4 v[104:107], v5, s[40:43], s58 offen sc1\n"
      "v_mfma_f32_16x16x32_bf16 v[40:43], a[80:83], v[80:83], v[40:43]\n"
      "v_mfma_f32_16x16x32_bf16 v[56:59], v[80:83], a[224:227], v[56:59]\n"
      "v_mfma_f32_16x16x32_bf16 v[44:47], a[80:83], v[84:87], v[44:47]\n"
      "v_mfma_f32_16x16x32_bf16 v[60:63], v[84:87], a[224:227], v[60:63]\n"
      "ds_read_b128 v[80:83], v9 offset:10240\n"
      "ds_read_b128 v[84:87], v9 offset:11264\n"
      "s_waitcnt lgkmcnt(4)\n"
      "v_mfma_f32_16x16x32_bf16 v[32:35], a[12:15], v[64:67], v[32:35]\n"
      "v_mfma_f32_16x16x32_bf16 v[48:51], v[64:67], a[156:159], v[48:51]\n"
      "v_mfma_f32_16x16x32_bf16 v[36:39], a[12:15], v[68:71], v[36:39]\n"
      "v_mfma_f32_16x16x32_bf16 v[52:55], v[68:71], a[156:159], v[52:55]\n"
      "buffer_store_dwordx4 v[108:111], v5, s[40:43], s59 offen sc1\n"
      "v_mfma_f32_16x16x32_bf16 v[40:43], a[84:87], v[64:67], v[40:43]\n"
      "v_mfma_f32_16x16x32_bf16 v[56:59], v[64:67], a[228:231], v[56:59]\n"
      "v_mfma_f32_16x16x32_bf16 v[44:47], a[84:87], v[68:71], v[44:47]\n"
      "v_mfma_f32_16x16x32_bf16 v[60:63], v[68:71], a[228:231], v[60:63]\n"
      "ds_read_b128 v[64:67], v9 offset:12288\n"
      "ds_read_b128 v[68:71], v9 offset:13312\n"
      "s_waitcnt lgkmcnt(4)\n"
      "v_mfma_f32_16x16x32_bf16 v[32:35], a[16:19], v[72:75], v[32:35]\n"
      "v_mfma_f32_16x16x32_bf16 v[48:51], v[72:75], a[160:163], v[48:51]\n"
      "v_mfma_f32_16x16x32_bf16 v[36:39], a[16:19], v[76:79], v[36:39]\n"
      "v_mfma_f32_16x16x32_bf16 v[52:55], v[76:79], a[160:163], v[52:55]\n"
      "s_add_u32 m0, s54, 0\n"
      "s_add_u32 s38, s53, 0\n"
      "buffer_load_dwordx4 v5, s[40:43], s38 offen sc1 lds\n"
      "v_mfma_f32_16x16x32_bf16 v[40:43], a[88:91], v[72:75], v[40:43]\n"
      "v_mfma_f32_16x16x32_bf16 v[56:59], v[72:75], a[232:235], v[56:59]\n"
      "v_mfma_f32_16x16x32_bf16 v[44:47], a[88:91], v[76:79], v[44:47]\n"
      "v_mfma_f32_16x16x32_bf16 v[60:63], v[76:79], a[232:235], v[60:63]\n"
      "ds_read_b128 v[72:75], v9 offset:14336\n"
      "ds_read_b128 v[76:79], v9 offset:15360\n"
      "s_waitcnt lgkmcnt(4)\n"
      "v_mfma_f32_16x16x32_bf16 v[32:35], a[20:23], v[80:83], v[32:35]\n"
      "v_mfma_f32_16x16x32_bf16 v[48:51], v[80:83], a[164:167], v[48:51]\n"
      "v_mfma_f32_16x16x32_bf16 v[36:39], a[20:23], v[84:87], v[36:39]\n"
      "v_mfma_f32_16x16x32_bf16 v[52:55], v[84:87], a[164:167], v[52:55]\n"
      "s_add_u32 m0, s54, 4096\n"
      "s_add_u32 s38, s53, 4096\n"
      "buffer_load_dwordx4 v5, s[40:43], s38 offen sc1 lds\n"
      "v_mfma_f32_16x16x32_bf16 v[40:43], a[92:95], v[80:83], v[40:43]\n"
      "v_mfma_f32_16x16x32_bf16 v[56:59], v[80:83], a[236:239], v[56:59]\n"
      "v_mfma_f32_16x16x32_bf16 v[44:47], a[92:95], v[84:87], v[44:47]\n"
      "v_mfma_f32_16x16x32_bf16 v[60:63], v[84:87], a[236:239], v[60:63]\n"
      "ds_read_b128 v[80:83], v9 offset:16384\n"
      "ds_read_b128 v[84:87], v9 offset:17408\n"
      "s_waitcnt lgkmcnt(4)\n"
      "v_mfma_f32_16x16x32_bf16 v[32:35], a[24:27], v[64:67], v[32:35]\n"
      "v_mfma_f32_16x16x32_bf16 v[48:51], v[64:67], a[168:171], v[48:51]\n"
      "v_mfma_f32_16x16x32_bf16 v[36:39], a[24:27], v[68:71], v[36:39]\n"
      "v_mfma_f32_16x16x32_bf16 v[52:55], v[68:71], a[168:171], v[52:55]\n"
      "s_add_u32 m0, s54, 8192\n"
      "s_add_u32 s38, s53, 8192\n"
      "buffer_load_dwordx4 v5, s[40:43], s38 offen sc1 lds\n"
      "v_mfma_f32_16x16x32_bf16 v[40:43], a[96:99], v[64:67], v[40:43]\n"
      "v_mfma_f32_16x16x32_bf16 v[56:59], v[64:67], a[240:243], v[56:59]\n"
      "v_mfma_f32_16x16x32_bf16 v[44:47], a[96:99], v[68:71], v[44:47]\n"
      "v_mfma_f32_16x16x32_bf16 v[60:63], v[68:71], a[240:243], v[60:63]\n"
      "ds_read_b128 v[64:67], v9 offset:18432\n"
      "ds_read_b128 v[68:71], v9 offset:19456\n"
      "s_waitcnt lgkmcnt(4)\n"
      "v_mfma_f32_16x16x32_bf16 v[32:35], a[28:31], v[72:75], v[32:35]\n"
      "v_mfma_f32_16x16x32_bf16 v[48:51], v[72:75], a[172:175], v[48:51]\n"
      "v_mfma_f32_16x16x32_bf16 v[36:39], a[28:31], v[76:79], v[36:39]\n"
      "v_mfma_f32_16x16x32_bf16 v[52:55], v[76:79], a[172:175], v[52:55]\n"
      "s_add_u32 m0, s54, 12288\n"
      "s_add_u32 s38, s53, 12288\n"
      "buffer_load_dwordx4 v5, s[40:43], s38 offen sc1 lds\n"
      "v_mfma_f32_16x16x32_bf16 v[40:43], a[100:103], v[72:75], v[40:43]\n"
      "v_mfma_f32_16x16x32_bf16 v[56:59], v[72:75], a[244:247], v[56:59]\n"
      "v_mfma_f32_16x16x32_bf16 v[44:47], a[100:103], v[76:79], v[44:47]\n"
      "v_mfma_f32_16x16x32_bf16 v[60:63], v[76:79], a[244:247], v[60:63]\n"
      "ds_read_b128 v[72:75], v9 offset:20480\n"
      "ds_read_b128 v[76:79], v9 offset:21504\n"
      "s_waitcnt lgkmcnt(4)\n"
      "v_mfma_f32_16x16x32_bf16 v[32:35], a[32:35], v[80:83], v[32:35]\n"
      "v_mfma_f32_16x16x32_bf16 v[48:51], v[80:83], a[176:179], v[48:51]\n"
      "v_mfma_f32_16x16x32_bf16 v[36:39], a[32:35], v[84:87], v[36:39]\n"
      "v_mfma_f32_16x16x32_bf16 v[52:55], v[84:87], a[176:179], v[52:55]\n"
      "s_add_u32 m0, s54, 16384\n"
      "s_add_u32 s38, s53, 16384\n"
      "buffer_load_dwordx4 v5, s[40:43], s38 offen sc1 lds\n"
      "v_mfma_f32_16x16x32_bf16 v[40:43], a[104:107], v[80:83], v[40:43]\n"
      "v_mfma_f32_16x16x32_bf16 v[56:59], v[80:83], a[248:251], v[56:59]\n"
      "v_mfma_f32_16x16x32_bf16 v[44:47], a[104:107], v[84:87], v[44:47]\n"
      "v_mfma_f32_16x16x32_bf16 v[60:63], v[84:87], a[248:251], v[60:63]\n"
      "ds_read_b128 v[80:83], v9 offset:22528\n"
      "ds_read_b128 v[84:87], v9 offset:23552\n"
      "s_waitcnt lgkmcnt(4)\n"
      "v_mfma_f32_16x16x32_bf16 v[32:35], a[36:39], v[64:67], v[32:35]\n"
      "v_mfma_f32_16x16x32_bf16 v[48:51], v[64:67], a[180:183], v[48:51]\n"
      "v_mfma_f32_16x16x32_bf16 v[36:39], a[36:39], v[68:71], v[36:39]\n"
      "v_mfma_f32_16x16x32_bf16 v[52:55], v[68:71], a[180:183], v[52:55]\n"
      "s_add_u32 m0, s54, 20480\n"
      "s_add_u32 s38, s53, 20480\n"
      "buffer_load_dwordx4 v5, s[40:43], s38 offen sc1 lds\n"
      "v_mfma_f32_16x16x32_bf16 v[40:43], a[108:111], v[64:67], v[40:43]\n"
      "v_mfma_f32_16x16x32_bf16 v[56:59], v[64:67], a[252:255], v[56:59]\n"
      "v_mfma_f32_16x16x32_bf16 v[44:47], a[108:111], v[68:71], v[44:47]\n"
      "v_mfma_f32_16x16x32_bf16 v[60:63], v[68:71], a[252:255], v[60:63]\n"
      "s_waitcnt vmcnt(6)\n"
      "s_barrier\n"
      "v_mov_b32 v12, s37\n"
      "buffer_store_dword v12, v8, s[40:43], 0 offen sc1\n"
      "ds_read_b128 v[64:67], v9 offset:24576\n"
      "ds_read_b128 v[68:71], v9 offset:25600\n"
      "s_waitcnt lgkmcnt(4)\n"
      "v_mfma_f32_16x16x32_bf16 v[32:35], a[40:43], v[72:75], v[32:35]\n"
      "v_mfma_f32_16x16x32_bf16 v[48:51], v[72:75], a[184:187], v[48:51]\n"
      "v_mfma_f32_16x16x32_bf16 v[36:39], a[40:43], v[76:79], v[36:39]\n"
      "v_mfma_f32_16x16x32_bf16 v[52:55], v[76:79], a[184:187], v[52:55]\n"
      "s_add_u32 m0, s54, 24576\n"
      "s_add_u32 s38, s53, 24576\n"
      "buffer_load_dwordx4 v5, s[40:43], s38 offen sc1 lds\n"
      "v_mfma_f32_16x16x32_bf16 v[40:43], a[112:115], v[72:75], v[40:43]\n"
      "v_mfma_f32_16x16x32_bf16 v[56:59], v[72:75], v[224:227], v[56:59]\n"
      "v_mfma_f32_16x16x32_bf16 v[44:47], a[112:115], v[76:79], v[44:47]\n"
      "v_mfma_f32_16x16x32_bf16 v[60:63], v[76:79], v[224:227], v[60:63]\n"
      "ds_read_b128 v[72:75], v9 offset:26624\n"
      "ds_read_b128 v[76:79], v9 offset:27648\n"
      "s_waitcnt lgkmcnt(4)\n"
      "v_mfma_f32_16x16x32_bf16 v[32:35], a[44:47], v[80:83], v[32:35]\n"
      "v_mfma_f32_16x16x32_bf16 v[48:51], v[80:83], a[188:191], v[48:51]\n"
      "v_mfma_f32_16x16x32_bf16 v[36:39], a[44:47], v[84:87], v[36:39]\n"
      "v_mfma_f32_16x16x32_bf16 v[52:55], v[84:87], a[188:191], v[52:55]\n"
      "s_add_u32 m0, s54, 28672\n"
      "s_add_u32 s38, s53, 28672\n"
      "buffer_load_dwordx4 v5, s[40:43], s38 offen sc1 lds\n"
      "v_mfma_f32_16x16x32_bf16 v[40:43], a[116:119], v[80:83], v[40:43]\n"
      "v_mfma_f32_16x16x32_bf16 v[56:59], v[80:83], v[228:231], v[56:59]\n"
      "v_mfma_f32_16x16x32_bf16 v[44:47], a[116:119], v[84:87], v[44:47]\n"
      "v_mfma_f32_16x16x32_bf16 v[60:63], v[84:87], v[228:231], v[60:63]\n"
      "ds_read_b128 v[80:83], v9 offset:28672\n"
      "ds_read_b128 v[84:87], v9 offset:29696\n"
      "s_waitcnt lgkmcnt(4)\n"
      "v_mfma_f32_16x16x32_bf16 v[32:35], a[48:51], v[64:67], v[32:35]\n"
      "v_mfma_f32_16x16x32_bf16 v[48:51], v[64:67], a[192:195], v[48:51]\n"
      "v_mfma_f32_16x16x32_bf16 v[36:39], a[48:51], v[68:71], v[36:39]\n"
      "v_mfma_f32_16x16x32_bf16 v[52:55], v[68:71], a[192:195], v[52:55]\n"
      "s_add_u32 m0, s54, 32768\n"
      "s_add_u32 s38, s53, 32768\n"
      "buffer_load_dwordx4 v5, s[40:43], s38 offen sc1 lds\n"
      "s_add_u32 s53, s53, 36864\n"
      "s_add_u32 s54, s54, 36864\n"
      "s_cmp_ge_u32 s54, 110592\n"
      "s_cselect_b32 s38, 110592, 0\n"
      "s_sub_u32 s54, s54, s38\n"
      "v_mfma_f32_16x16x32_bf16 v[40:43], a[120:123], v[64:67], v[40:43]\n"
      "v_mfma_f32_16x16x32_bf16 v[56:59], v[64:67], v[232:235], v[56:59]\n"
      "v_mfma_f32_16x16x32_bf16 v[44:47], a[120:123], v[68:71], v[44:47]\n"
      "v_mfma_f32_16x16x32_bf16 v[60:63], v[68:71], v[232:235], v[60:63]\n"
      "ds_read_b128 v[64:67], v9 offset:30720\n"
      "ds_read_b128 v[68:71], v9 offset:31744\n"
      "s_waitcnt lgkmcnt(4)\n"
      "v_mfma_f32_16x16x32_bf16 v[32:35], a[52:55], v[72:75], v[32:35]\n"
      "v_mfma_f32_16x16x32_bf16 v[48:51], v[72:75], a[196:199], v[48:51]\n"
      "v_mfma_f32_16x16x32_bf16 v[36:39], a[52:55], v[76:79], v[36:39]\n"
      "v_mfma_f32_16x16x32_bf16 v[52:55], v[76:79], a[196:199], v[52:55]\n"
      "v_mfma_f32_16x16x32_bf16 v[40:43], a[124:127], v[72:75], v[40:43]\n"
      "v_mfma_f32_16x16x32_bf16 v[56:59], v[72:75], v[236:239], v[56:59]\n"
      "v_mfma_f32_16x16x32_bf16 v[44:47], a[124:127], v[76:79], v[44:47]\n"
      "v_mfma_f32_16x16x32_bf16 v[60:63], v[76:79], v[236:239], v[60:63]\n"
      "ds_read_b128 v[72:75], v9 offset:32768\n"
      "ds_read_b128 v[76:79], v9 offset:33792\n"
      "s_waitcnt lgkmcnt(4)\n"
      "v_mfma_f32_16x16x32_bf16 v[32:35], a[56:59], v[80:83], v[32:35]\n"
      "v_mfma_f32_16x16x32_bf16 v[48:51], v[80:83], a[200:203], v[48:51]\n"
      "v_mfma_f32_16x16x32_bf16 v[36:39], a[56:59], v[84:87], v[36:39]\n"
      "v_mfma_f32_16x16x32_bf16 v[52:55], v[84:87], a[200:203], v[52:55]\n"
      "v_mfma_f32_16x16x32_bf16 v[40:43], a[128:131], v[80:83], v[40:43]\n"
      "v_mfma_f32_16x16x32_bf16 v[56:59], v[80:83], v[240:243], v[56:59]\n"
      "v_mfma_f32_16x16x32_bf16 v[44:47], a[128:131], v[84:87], v[44:47]\n"
      "v_mfma_f32_16x16x32_bf16 v[60:63], v[84:87], v[240:243], v[60:63]\n"
      "ds_read_b128 v[80:83], v9 offset:34816\n"
      "ds_read_b128 v[84:87], v9 offset:35840\n"
      "s_waitcnt lgkmcnt(4)\n"
      "v_mfma_f32_16x16x32_bf16 v[32:35], a[60:63], v[64:67], v[32:35]\n"
      "v_mfma_f32_16x16x32_bf16 v[48:51], v[64:67], a[204:207], v[48:51]\n"
      "v_mfma_f32_16x16x32_bf16 v[36:39], a[60:63], v[68:71], v[36:39]\n"
      "v_mfma_f32_16x16x32_bf16 v[52:55], v[68:71], a[204:207], v[52:55]\n"
      "v_mfma_f32_16x16x32_bf16 v[40:43], a[132:135], v[64:67], v[40:43]\n"
      "v_mfma_f32_16x16x32_bf16 v[56:59], v[64:67], v[244:247], v[56:59]\n"
      "v_mfma_f32_16x16x32_bf16 v[44:47], a[132:135], v[68:71], v[44:47]\n"
      "v_mfma_f32_16x16x32_bf16 v[60:63], v[68:71], v[244:247], v[60:63]\n"
      "s_waitcnt lgkmcnt(2)\n"
      "v_mfma_f32_16x16x32_bf16 v[32:35], a[64:67], v[72:75], v[32:35]\n"
      "v_mfma_f32_16x16x32_bf16 v[48:51], v[72:75], a[208:211], v[48:51]\n"
      "v_mfma_f32_16x16x32_bf16 v[36:39], a[64:67], v[76:79], v[36:39]\n"
      "v_mfma_f32_16x16x32_bf16 v[52:55], v[76:79], a[208:211], v[52:55]\n"
      "v_mfma_f32_16x16x32_bf16 v[40:43], a[136:139], v[72:75], v[40:43]\n"
      "v_mfma_f32_16x16x32_bf16 v[56:59], v[72:75], v[248:251], v[56:59]\n"
      "v_mfma_f32_16x16x32_bf16 v[44:47], a[136:139], v[76:79], v[44:47]\n"
      "v_mfma_f32_16x16x32_bf16 v[60:63], v[76:79], v[248:251], v[60:63]\n"
      "s_waitcnt lgkmcnt(0)\n"
      "v_mfma_f32_16x16x32_bf16 v[32:35], a[68:71], v[80:83], v[32:35]\n"
      "v_mfma_f32_16x16x32_bf16 v[48:51], v[80:83], a[212:215], v[48:51]\n"
      "v_mfma_f32_16x16x32_bf16 v[36:39], a[68:71], v[84:87], v[36:39]\n"
      "v_mfma_f32_16x16x32_bf16 v[52:55], v[84:87], a[212:215], v[52:55]\n"
      "v_mfma_f32_16x16x32_bf16 v[40:43], a[140:143], v[80:83], v[40:43]\n"
      "v_mfma_f32_16x16x32_bf16 v[56:59], v[80:83], v[252:255], v[56:59]\n"
      "v_mfma_f32_16x16x32_bf16 v[44:47], a[140:143], v[84:87], v[44:47]\n"
      "v_mfma_f32_16x16x32_bf16 v[60:63], v[84:87], v[252:255], v[60:63]\n"
      "s_add_u32 s55, s55, 36864\n"
      "s_cmp_ge_u32 s55, 110592\n"
      "s_cselect_b32 s55, 0, s55\n"
      "s_add_u32 s57, s56, s36\n"
      "s_add_u32 s58, s56, 524288\n"
      "s_add_u32 s59, s58, 4096\n"
      "s_add_u32 s37, s37, 1\n"
      "s_nop 1\n"
      "v_cvt_pk_bf16_f32 v96, v32, v33\n"
      "v_cvt_pk_bf16_f32 v97, v34, v35\n"
      "v_cvt_pk_bf16_f32 v98, v40, v41\n"
      "v_cvt_pk_bf16_f32 v99, v42, v43\n"
      "v_cvt_pk_bf16_f32 v100, v36, v37\n"
      "v_cvt_pk_bf16_f32 v101, v38, v39\n"
      "v_cvt_pk_bf16_f32 v102, v44, v45\n"
      "v_cvt_pk_bf16_f32 v103, v46, v47\n"
      "v_cvt_pk_bf16_f32 v104, v48, v49\n"
      "v_cvt_pk_bf16_f32 v105, v50, v51\n"
      "v_cvt_pk_bf16_f32 v106, v52, v53\n"
      "v_cvt_pk_bf16_f32 v107, v54, v55\n"
      "v_cvt_pk_bf16_f32 v108, v56, v57\n"
      "v_cvt_pk_bf16_f32 v109, v58, v59\n"
      "v_cvt_pk_bf16_f32 v110, v60, v61\n"
      "v_cvt_pk_bf16_f32 v111, v62, v63\n"
      "s_add_u32 s56, s56, 1048576\n"
      "s_lshr_b32 s39, s56, 8\n"
      "s_xor_b32 s39, s39, s90\n"
      "s_and_b32 s39, s39, 8192\n"
      "s_bitset0_b32 s56, 13\n"
      "s_or_b32 s56, s56, s39\n"
      "s_cmp_lt_u32 s37, 64\n"
      "s_cbranch_scc1 kvtop51_%=\n"
      "buffer_store_dwordx4 v[96:99], v5, s[40:43], s57 offen offset:0 sc1\n"
      "buffer_store_dwordx4 v[100:103], v5, s[40:43], s57 offen offset:1024 sc1\n"
      "buffer_store_dwordx4 v[104:107], v5, s[40:43], s58 offen sc1\n"
      "buffer_store_dwordx4 v[108:111], v5, s[40:43], s59 offen sc1\n"
      "s_waitcnt vmcnt(0)\n"
      "s_barrier\n"
      "v_mov_b32 v12, s37\n"
      "buffer_store_dword v12, v8, s[40:43], 0 offen sc1\n"
      "s_waitcnt vmcnt(0)\n"
      "s_endpgm\n"
      :: "{v0}"(tid) : "memory", "v255", "a255", "s99");
    return;
  }
  if (kind < 3) {
    const int qt = kind - 1;
    {
      const int seg = h * 2 + qt, r = 8 * w + (l & 7), c8 = l >> 3;
      const unsigned found = __builtin_amdgcn_raw_buffer_load_b32(ws, w == 0 ? hsa : OOB, 0, 16);
      v4u xv[9];
#pragma unroll
      for (int j = 0; j < 9; ++j) xv[j] = *(const v4u*)(p.x + (seg * 32 + r) * 576 + (j * 8 + c8) * 8);
      __builtin_amdgcn_sched_barrier(0);
      const unsigned word = greet_post(ws, found, tid);
#pragma unroll
      for (int pass = 0; pass < 2; ++pass) {
        float s = 0.f;
#pragma unroll
        for (int j = 0; j < 9; ++j)
#pragma unroll
          for (int i = 0; i < 4; ++i) s += blo(xv[j][i]) + bhi(xv[j][i]);
        s += __shfl_xor(s, 8); s += __shfl_xor(s, 16); s += __shfl_xor(s, 32);
        const float m = s * (1.0f / 576.0f);
        float v = 0.f;
#pragma unroll
        for (int j = 0; j < 9; ++j)
#pragma unroll
          for (int i = 0; i < 4; ++i) { const float a = blo(xv[j][i]) - m, b = bhi(xv[j][i]) - m; v += a * a + b * b; }
        v += __shfl_xor(v, 8); v += __shfl_xor(v, 16); v += __shfl_xor(v, 32);
        const float rs = 1.0f / sqrtf(v * (1.0f / 576.0f) + 1e-5f);
#pragma unroll
        for (int j = 0; j < 9; ++j) {
#pragma unroll
          for (int i = 0; i < 4; ++i) xv[j][i] = pk2((blo(xv[j][i]) - m) * rs, (bhi(xv[j][i]) - m) * rs);
          const int ci = j * 8 + c8;
          if (pass == 0) __builtin_amdgcn_raw_buffer_store_b128(xv[j], ws, XNF + seg * 36864 + (((ci >> 2) * 2 + (r >> 4)) * 64 + (ci & 3) * 16 + (r & 15)) * 16, 0, 16);
          else *LDSP(v4u, r * 1168 + ci * 16) = xv[j];
        }
      }
      __syncthreads();
#pragma unroll
      for (int i = 0; i < 9; ++i) {
        const int t = 9 * w + i, a = (8 * g + q) * 1168 + (32 * (t >> 1) + 8 * pp + 4 * (t & 1)) * 2;
        const v2u r0 = __builtin_bit_cast(v2u, __builtin_amdgcn_ds_read_tr16_b64_v4i16(LDSP(v4s, a)));
        const v2u r1 = __builtin_bit_cast(v2u, __builtin_amdgcn_ds_read_tr16_b64_v4i16(LDSP(v4s, a + 4 * 1168)));
        const v4u o = {r0.x, r0.y, r1.x, r1.y};
        __builtin_amdgcn_raw_buffer_store_b128(o, ws, H2F + seg * 36864 + t * 1024 + l16, 0, 16);
      }
      if (w == 0) greet_wait(ws, word, l, budget);
      publish(ws, LNF + seg * 128, tid);
    }
    if (XP & 8) return;
    const int p0 = (0xE950 >> (4 * w)) & 15, px = w < 2 ? 4 : 13;
    TAB(TB, 0) = wdma; TAB(TB, 1) = tr0; TAB(TB, 2) = tr1; TAB(TB, 3) = g1; TAB(TB, 4) = l16;
    TAB(TB, 5) = LNF + l * 128; TAB(TB, 6) = tid == 0 ? QOP + (qt * 32 + h) * 128 : OOB;
    TAB(TB, 7) = (4 * g + q) * 1152 + p0 * 64 + pp * 16;
    TAB(TB, 8) = (4 * g + q) * 1152 + px * 64 + pp * 16 + (w & 1) * 8;
    TAB(TB, 9) = (XP & 2) ? OOB : (h >> 3) * 16384 + (h & 7) * 1024 + l16;
    TAB(TB, 10) = (XP & 6) ? OOB : (h >> 3) * 16384 + (h & 7) * 1024 + l * 8 + (w & 1) * 512;
    TAB(TB, 11) = 16 * qt + c - 4 * g;
    TAB(TB, 12) = (PART / 65536 + qt * 18 + p0) << 16 | (PART / 65536 + qt * 18 + px); TAB(TB, 13) = h * 128 + (l < 2 ? CKP + (w >> 1) * 16384 + (w & 1) * 4096 + l * 64 : KVP);
    TAB(TB, 14) = w * 2048 + l16; TAB(TB, 15) = g3;
    TAB(TB, 16) = lo32(p.ws); TAB(TB, 17) = hi32(p.ws); TAB(TB, 18) = lo32(p.wq); TAB(TB, 19) = hi32(p.wq);
    TAB(TB, 20) = lo32(p.wo + h * 128 * 576); TAB(TB, 21) = hi32(p.wo + h * 128 * 576);
    TAB(TB, 22) = w * 1024; TAB(TB, 23) = h | qt << 8;
    asm volatile("s_waitcnt lgkmcnt(0)\n"
      "v_lshlrev_b32 v1, 2, v0\n"
      "v_add_u32 v1, 131072, v1\n"
      "ds_read_b32 v2, v1 offset:0\n"
      "ds_read_b32 v3, v1 offset:1024\n"
      "ds_read_b32 v4, v1 offset:2048\n"
      "ds_read_b32 v5, v1 offset:3072\n"
      "ds_read_b32 v6, v1 offset:4096\n"
      "ds_read_b32 v7, v1 offset:5120\n"
      "ds_read_b32 v8, v1 offset:6144\n"
      "ds_read_b32 v9, v1 offset:7168\n"
      "s_waitcnt lgkmcnt(0)\n"
      "ds_read_b32 v10, v1 offset:8192\n"
      "ds_read_b32 v11, v1 offset:9216\n"
      "ds_read_b32 v12, v1 offset:10240\n"
      "ds_read_b32 v13, v1 offset:11264\n"
      "ds_read_b32 v14, v1 offset:12288\n"
      "ds_read_b32 v15, v1 offset:13312\n"
      "ds_read_b32 v16, v1 offset:14336\n"
      "ds_read_b32 v17, v1 offset:15360\n"
      "s_waitcnt lgkmcnt(0)\n"
      "ds_read_b32 v32, v1 offset:16384\n"
      "ds_read_b32 v33, v1 offset:17408\n"
      "ds_read_b32 v34, v1 offset:18432\n"
      "ds_read_b32 v35, v1 offset:19456\n"
      "ds_read_b32 v36, v1 offset:20480\n"
      "ds_read_b32 v37, v1 offset:21504\n"
      "ds_read_b32 v38, v1 offset:22528\n"
      "ds_read_b32 v39, v1 offset:23552\n"
      "s_waitcnt lgkmcnt(0)\n"
      "v_readfirstlane_b32 s40, v32\n"
      "v_readfirstlane_b32 s41, v33\n"
      "v_readfirstlane_b32 s48, v34\n"
      "v_readfirstlane_b32 s49, v35\n"
      "v_readfirstlane_b32 s50, v36\n"
      "v_readfirstlane_b32 s51, v37\n"
      "v_readfirstlane_b32 s36, v38\n"
      "v_readfirstlane_b32 s52, v39\n"
      "s_nop 4\n"
      "s_mov_b32 s42, 226492416\n"
      "s_mov_b32 s43, 0x20000\n"
      "s_mov_b32 s46, 4718592\n"
      "s_mov_b32 s47, 0x20000\n"
      "s_mov_b32 s98, 0\n"
      "s_mov_b32 s44, s48\n"
      "s_mov_b32 s45, s49\n"
      "s_add_u32 m0, s36, 0\n"
      "s_mov_b32 s38, 0\n"
      "buffer_load_dwordx4 v2, s[44:47], s38 offen lds\n"
      "s_add_u32 m0, s36, 4096\n"
      "s_mov_b32 s38, 131072\n"
      "buffer_load_dwordx4 v2, s[44:47], s38 offen lds\n"
      "s_add_u32 m0, s36, 8192\n"
      "s_mov_b32 s38, 262144\n"
      "buffer_load_dwordx4 v2, s[44:47], s38 offen lds\n"
      "s_add_u32 m0, s36, 12288\n"
      "s_mov_b32 s38, 393216\n"
      "buffer_load_dwordx4 v2, s[44:47], s38 offen lds\n"
      "s_add_u32 m0, s36, 16384\n"
      "s_mov_b32 s38, 524288\n"
      "buffer_load_dwordx4 v2, s[44:47], s38 offen lds\n"
      "s_add_u32 m0, s36, 20480\n"
      "s_mov_b32 s38, 655360\n"
      "buffer_load_dwordx4 v2, s[44:47], s38 offen lds\n"
      "s_add_u32 m0, s36, 24576\n"
      "s_mov_b32 s38, 786432\n"
      "buffer_load_dwordx4 v2, s[44:47], s38 offen lds\n"
      "s_add_u32 m0, s36, 28672\n"
      "s_mov_b32 s38, 917504\n"
      "buffer_load_dwordx4 v2, s[44:47], s38 offen lds\n"
      "s_add_u32 m0, s36, 32768\n"
      "s_mov_b32 s38, 1048576\n"
      "buffer_load_dwordx4 v2, s[44:47], s38 offen lds\n"
      "s_add_u32 m0, s36, 36864\n"
      "s_mov_b32 s38, 1179648\n"
      "buffer_load_dwordx4 v2, s[44:47], s38 offen lds\n"
      "s_add_u32 m0, s36, 40960\n"
      "s_mov_b32 s38, 1310720\n"
      "buffer_load_dwordx4 v2, s[44:47], s38 offen lds\n"
      "s_add_u32 m0, s36, 45056\n"
      "s_mov_b32 s38, 1441792\n"
      "buffer_load_dwordx4 v2, s[44:47], s38 offen lds\n"
      "s_add_u32 m0, s36, 49152\n"
      "s_mov_b32 s38, 1572864\n"
      "buffer_load_dwordx4 v2, s[44:47], s38 offen lds\n"
      "s_add_u32 m0, s36, 53248\n"
      "s_mov_b32 s38, 1703936\n"
      "buffer_load_dwordx4 v2, s[44:47], s38 offen lds\n"
      "s_add_u32 m0, s36, 57344\n"
      "s_mov_b32 s38, 1835008\n"
      "buffer_load_dwordx4 v2, s[44:47], s38 offen lds\n"
      "s_add_u32 m0, s36, 61440\n"
      "s_mov_b32 s38, 1966080\n"
      "buffer_load_dwordx4 v2, s[44:47], s38 offen lds\n"
      "s_add_u32 m0, s36, 65536\n"
      "s_mov_b32 s38, 2097152\n"
      "buffer_load_dwordx4 v2, s[44:47], s38 offen lds\n"
      "s_add_u32 m0, s36, 69632\n"
      "s_mov_b32 s38, 2228224\n"
      "buffer_load_dwordx4 v2, s[44:47], s38 offen lds\n"
      "s_add_u32 m0, s36, 73728\n"
      "s_mov_b32 s38, 2359296\n"
      "buffer_load_dwordx4 v2, s[44:47], s38 offen lds\n"
      "s_add_u32 m0, s36, 77824\n"
      "s_mov_b32 s38, 2490368\n"
      "buffer_load_dwordx4 v2, s[44:47], s38 offen lds\n"
      "s_add_u32 m0, s36, 81920\n"
      "s_mov_b32 s38, 2621440\n"
      "buffer_load_dwordx4 v2, s[44:47], s38 offen lds\n"
      "s_add_u32 m0, s36, 86016\n"
      "s_mov_b32 s38, 2752512\n"
      "buffer_load_dwordx4 v2, s[44:47], s38 offen lds\n"
      "s_add_u32 m0, s36, 90112\n"
      "s_mov_b32 s38, 2883584\n"
      "buffer_load_dwordx4 v2, s[44:47], s38 offen lds\n"
      "s_add_u32 m0, s36, 94208\n"
      "s_mov_b32 s38, 3014656\n"
      "buffer_load_dwordx4 v2, s[44:47], s38 offen lds\n"
      "s_add_u32 m0, s36, 98304\n"
      "s_mov_b32 s38, 3145728\n"
      "buffer_load_dwordx4 v2, s[44:47], s38 offen lds\n"
      "s_add_u32 m0, s36, 102400\n"
      "s_mov_b32 s38, 3276800\n"
      "buffer_load_dwordx4 v2, s[44:47], s38 offen lds\n"
      "s_add_u32 m0, s36, 106496\n"
      "s_mov_b32 s38, 3407872\n"
      "buffer_load_dwordx4 v2, s[44:47], s38 offen lds\n"
      "s_add_u32 m0, s36, 110592\n"
      "s_mov_b32 s38, 3538944\n"
      "buffer_load_dwordx4 v2, s[44:47], s38 offen lds\n"
      "s_waitcnt vmcnt(24)\n"
      "s_barrier\n"
      "s_add_u32 m0, s36, 114688\n"
      "s_mov_b32 s38, 3670016\n"
      "buffer_load_dwordx4 v2, s[44:47], s38 offen lds\n"
      "s_add_u32 m0, s36, 118784\n"
      "s_mov_b32 s38, 3801088\n"
      "buffer_load_dwordx4 v2, s[44:47], s38 offen lds\n"
      "s_add_u32 m0, s36, 122880\n"
      "s_mov_b32 s38, 3932160\n"
      "buffer_load_dwordx4 v2, s[44:47], s38 offen lds\n"
      "s_add_u32 m0, s36, 126976\n"
      "s_mov_b32 s38, 4063232\n"
      "buffer_load_dwordx4 v2, s[44:47], s38 offen lds\n"
      "v_add_u32 v20, 0, v3\n"
      "v_add_u32 v21, 0, v4\n"
      "ds_read_b64_tr_b16 a[0:1], v20 offset:0\n"
      "ds_read_b64_tr_b16 a[2:3], v20 offset:1024\n"
      "ds_read_b64_tr_b16 a[4:5], v20 offset:8192\n"
      "ds_read_b64_tr_b16 a[6:7], v20 offset:9216\n"
      "ds_read_b64_tr_b16 a[72:73], v21 offset:0\n"
      "ds_read_b64_tr_b16 a[74:75], v21 offset:1024\n"
      "ds_read_b64_tr_b16 a[76:77], v21 offset:8192\n"
      "ds_read_b64_tr_b16 a[78:79], v21 offset:9216\n"
      "s_waitcnt lgkmcnt(0)\n"
      "s_waitcnt vmcnt(24)\n"
      "s_barrier\n"
      "s_add_u32 m0, s36, 0\n"
      "s_mov_b32 s38, 4194304\n"
      "buffer_load_dwordx4 v2, s[44:47], s38 offen lds\n"
      "s_add_u32 m0, s36, 4096\n"
      "s_mov_b32 s38, 4325376\n"
      "buffer_load_dwordx4 v2, s[44:47], s38 offen lds\n"
      "s_add_u32 m0, s36, 8192\n"
      "s_mov_b32 s38, 4456448\n"
      "buffer_load_dwordx4 v2, s[44:47], s38 offen lds\n"
      "s_add_u32 m0, s36, 12288\n"
      "s_mov_b32 s38, 4587520\n"
      "buffer_load_dwordx4 v2, s[44:47], s38 offen lds\n"
      "v_add_u32 v20, 16384, v3\n"
      "v_add_u32 v21, 16384, v4\n"
      "ds_read_b64_tr_b16 a[8:9], v20 offset:0\n"
      "ds_read_b64_tr_b16 a[10:11], v20 offset:1024\n"
      "ds_read_b64_tr_b16 a[12:13], v20 offset:8192\n"
      "ds_read_b64_tr_b16 a[14:15], v20 offset:9216\n"
      "ds_read_b64_tr_b16 a[80:81], v21 offset:0\n"
      "ds_read_b64_tr_b16 a[82:83], v21 offset:1024\n"
      "ds_read_b64_tr_b16 a[84:85], v21 offset:8192\n"
      "ds_read_b64_tr_b16 a[86:87], v21 offset:9216\n"
      "s_waitcnt lgkmcnt(0)\n"
      "s_waitcnt vmcnt(24)\n"
      "s_barrier\n"
      "v_add_u32 v20, 32768, v3\n"
      "v_add_u32 v21, 32768, v4\n"
      "ds_read_b64_tr_b16 a[16:17], v20 offset:0\n"
      "ds_read_b64_tr_b16 a[18:19], v20 offset:1024\n"
      "ds_read_b64_tr_b16 a[20:21], v20 offset:8192\n"
      "ds_read_b64_tr_b16 a[22:23], v20 offset:9216\n"
      "ds_read_b64_tr_b16 a[88:89], v21 offset:0\n"
      "ds_read_b64_tr_b16 a[90:91], v21 offset:1024\n"
      "ds_read_b64_tr_b16 a[92:93], v21 offset:8192\n"
      "ds_read_b64_tr_b16 a[94:95], v21 offset:9216\n"
      "s_waitcnt lgkmcnt(0)\n"
      "s_waitcnt vmcnt(20)\n"
      "s_barrier\n"
      "v_add_u32 v20, 49152, v3\n"
      "v_add_u32 v21, 49152, v4\n"
      "ds_read_b64_tr_b16 a[24:25], v20 offset:0\n"
      "ds_read_b64_tr_b16 a[26:27], v20 offset:1024\n"
      "ds_read_b64_tr_b16 a[28:29], v20 offset:8192\n"
      "ds_read_b64_tr_b16 a[30:31], v20 offset:9216\n"
      "ds_read_b64_tr_b16 a[96:97], v21 offset:0\n"
      "ds_read_b64_tr_b16 a[98:99], v21 offset:1024\n"
      "ds_read_b64_tr_b16 a[100:101], v21 offset:8192\n"
      "ds_read_b64_tr_b16 a[102:103], v21 offset:9216\n"
      "s_waitcnt lgkmcnt(0)\n"
      "s_waitcnt vmcnt(16)\n"
      "s_barrier\n"
      "v_add_u32 v20, 65536, v3\n"
      "v_add_u32 v21, 65536, v4\n"
      "ds_read_b64_tr_b16 a[32:33], v20 offset:0\n"
      "ds_read_b64_tr_b16 a[34:35], v20 offset:1024\n"
      "ds_read_b64_tr_b16 a[36:37], v20 offset:8192\n"
      "ds_read_b64_tr_b16 a[38:39], v20 offset:9216\n"
      "ds_read_b64_tr_b16 a[104:105], v21 offset:0\n"
      "ds_read_b64_tr_b16 a[106:107], v21 offset:1024\n"
      "ds_read_b64_tr_b16 a[108:109], v21 offset:8192\n"
      "ds_read_b64_tr_b16 a[110:111], v21 offset:9216\n"
      "s_waitcnt lgkmcnt(0)\n"
      "s_waitcnt vmcnt(12)\n"
      "s_barrier\n"
      "v_add_u32 v20, 81920, v3\n"
      "v_add_u32 v21, 81920, v4\n"
      "ds_read_b64_tr_b16 a[40:41], v20 offset:0\n"
      "ds_read_b64_tr_b16 a[42:43], v20 offset:1024\n"
      "ds_read_b64_tr_b16 a[44:45], v20 offset:8192\n"
      "ds_read_b64_tr_b16 a[46:47], v20 offset:9216\n"
      "ds_read_b64_tr_b16 a[112:113], v21 offset:0\n"
      "ds_read_b64_tr_b16 a[114:115], v21 offset:1024\n"
      "ds_read_b64_tr_b16 a[116:117], v21 offset:8192\n"
      "ds_read_b64_tr_b16 a[118:119], v21 offset:9216\n"
      "s_waitcnt lgkmcnt(0)\n"
      "s_waitcnt vmcnt(8)\n"
      "s_barrier\n"
      "v_add_u32 v20, 98304, v3\n"
      "v_add_u32 v21, 98304, v4\n"
      "ds_read_b64_tr_b16 a[48:49], v20 offset:0\n"
      "ds_read_b64_tr_b16 a[50:51], v20 offset:1024\n"
      "ds_read_b64_tr_b16 a[52:53], v20 offset:8192\n"
      "ds_read_b64_tr_b16 a[54:55], v20 offset:9216\n"
      "ds_read_b64_tr_b16 a[120:121], v21 offset:0\n"
      "ds_read_b64_tr_b16 a[122:123], v21 offset:1024\n"
      "ds_read_b64_tr_b16 a[124:125], v21 offset:8192\n"
      "ds_read_b64_tr_b16 a[126:127], v21 offset:9216\n"
      "s_waitcnt lgkmcnt(0)\n"
      "s_waitcnt vmcnt(4)\n"
      "s_barrier\n"
      "v_add_u32 v20, 114688, v3\n"
      "v_add_u32 v21, 114688, v4\n"
      "ds_read_b64_tr_b16 a[56:57], v20 offset:0\n"
      "ds_read_b64_tr_b16 a[58:59], v20 offset:1024\n"
      "ds_read_b64_tr_b16 a[60:61], v20 offset:8192\n"
      "ds_read_b64_tr_b16 a[62:63], v20 offset:9216\n"
      "ds_read_b64_tr_b16 a[128:129], v21 offset:0\n"
      "ds_read_b64_tr_b16 a[130:131], v21 offset:1024\n"
      "ds_read_b64_tr_b16 a[132:133], v21 offset:8192\n"
      "ds_read_b64_tr_b16 a[134:135], v21 offset:9216\n"
      "s_waitcnt lgkmcnt(0)\n"
      "s_waitcnt vmcnt(0)\n"
      "s_barrier\n"
      "v_add_u32 v20, 0, v3\n"
      "v_add_u32 v21, 0, v4\n"
      "ds_read_b64_tr_b16 a[64:65], v20 offset:0\n"
      "ds_read_b64_tr_b16 a[66:67], v20 offset:1024\n"
      "ds_read_b64_tr_b16 a[68:69], v20 offset:8192\n"
      "ds_read_b64_tr_b16 a[70:71], v20 offset:9216\n"
      "ds_read_b64_tr_b16 a[136:137], v21 offset:0\n"
      "ds_read_b64_tr_b16 a[138:139], v21 offset:1024\n"
      "ds_read_b64_tr_b16 a[140:141], v21 offset:8192\n"
      "ds_read_b64_tr_b16 a[142:143], v21 offset:9216\n"
      "s_waitcnt lgkmcnt(0)\n"
      "s_barrier\n"
      "s_mov_b32 s44, s50\n"
      "s_mov_b32 s45, s51\n"
      "s_mov_b32 s46, 147456\n"
      "s_add_u32 m0, s36, 0\n"
      "s_mov_b32 s38, 0\n"
      "buffer_load_dwordx4 v5, s[44:47], s38 offen lds\n"
      "s_add_u32 m0, s36, 4096\n"
      "s_mov_b32 s38, 4096\n"
      "buffer_load_dwordx4 v5, s[44:47], s38 offen lds\n"
      "s_add_u32 m0, s36, 8192\n"
      "s_mov_b32 s38, 8192\n"
      "buffer_load_dwordx4 v5, s[44:47], s38 offen lds\n"
      "s_add_u32 m0, s36, 12288\n"
      "s_mov_b32 s38, 12288\n"
      "buffer_load_dwordx4 v5, s[44:47], s38 offen lds\n"
      "s_add_u32 m0, s36, 16384\n"
      "s_mov_b32 s38, 16384\n"
      "buffer_load_dwordx4 v5, s[44:47], s38 offen lds\n"
      "s_add_u32 m0, s36, 20480\n"
      "s_mov_b32 s38, 20480\n"
      "buffer_load_dwordx4 v5, s[44:47], s38 offen lds\n"
      "s_add_u32 m0, s36, 24576\n"
      "s_mov_b32 s38, 24576\n"
      "buffer_load_dwordx4 v5, s[44:47], s38 offen lds\n"
      "s_add_u32 m0, s36, 28672\n"
      "s_mov_b32 s38, 28672\n"
      "buffer_load_dwordx4 v5, s[44:47], s38 offen lds\n"
      "s_add_u32 m0, s36, 32768\n"
      "s_mov_b32 s38, 32768\n"
      "buffer_load_dwordx4 v5, s[44:47], s38 offen lds\n"
      "s_add_u32 m0, s36, 36864\n"
      "s_mov_b32 s38, 36864\n"
      "buffer_load_dwordx4 v5, s[44:47], s38 offen lds\n"
      "s_add_u32 m0, s36, 40960\n"
      "s_mov_b32 s38, 40960\n"
      "buffer_load_dwordx4 v5, s[44:47], s38 offen lds\n"
      "s_add_u32 m0, s36, 45056\n"
      "s_mov_b32 s38, 45056\n"
      "buffer_load_dwordx4 v5, s[44:47], s38 offen lds\n"
      "s_add_u32 m0, s36, 49152\n"
      "s_mov_b32 s38, 49152\n"
      "buffer_load_dwordx4 v5, s[44:47], s38 offen lds\n"
      "s_add_u32 m0, s36, 53248\n"
      "s_mov_b32 s38, 53248\n"
      "buffer_load_dwordx4 v5, s[44:47], s38 offen lds\n"
      "s_add_u32 m0, s36, 57344\n"
      "s_mov_b32 s38, 57344\n"
      "buffer_load_dwordx4 v5, s[44:47], s38 offen lds\n"
      "s_add_u32 m0, s36, 61440\n"
      "s_mov_b32 s38, 61440\n"
      "buffer_load_dwordx4 v5, s[44:47], s38 offen lds\n"
      "s_add_u32 m0, s36, 65536\n"
      "s_mov_b32 s38, 65536\n"
      "buffer_load_dwordx4 v5, s[44:47], s38 offen lds\n"
      "s_add_u32 m0, s36, 69632\n"
      "s_mov_b32 s38, 69632\n"
      "buffer_load_dwordx4 v5, s[44:47], s38 offen lds\n"
      "s_add_u32 m0, s36, 73728\n"
      "s_mov_b32 s38, 73728\n"
      "buffer_load_dwordx4 v5, s[44:47], s38 offen lds\n"
      "s_add_u32 m0, s36, 77824\n"
      "s_mov_b32 s38, 77824\n"
      "buffer_load_dwordx4 v5, s[44:47], s38 offen lds\n"
      "s_add_u32 m0, s36, 81920\n"
      "s_mov_b32 s38, 81920\n"
      "buffer_load_dwordx4 v5, s[44:47], s38 offen lds\n"
      "s_add_u32 m0, s36, 86016\n"
      "s_mov_b32 s38, 86016\n"
      "buffer_load_dwordx4 v5, s[44:47], s38 offen lds\n"
      "s_add_u32 m0, s36, 90112\n"
      "s_mov_b32 s38, 90112\n"
      "buffer_load_dwordx4 v5, s[44:47], s38 offen lds\n"
      "s_add_u32 m0, s36, 94208\n"
      "s_mov_b32 s38, 94208\n"
      "buffer_load_dwordx4 v5, s[44:47], s38 offen lds\n"
      "s_add_u32 m0, s36, 98304\n"
      "s_mov_b32 s38, 98304\n"
      "buffer_load_dwordx4 v5, s[44:47], s38 offen lds\n"
      "s_add_u32 m0, s36, 102400\n"
      "s_mov_b32 s38, 102400\n"
      "buffer_load_dwordx4 v5, s[44:47], s38 offen lds\n"
      "s_add_u32 m0, s36, 106496\n"
      "s_mov_b32 s38, 106496\n"
      "buffer_load_dwordx4 v5, s[44:47], s38 offen lds\n"
      "s_add_u32 m0, s36, 110592\n"
      "s_mov_b32 s38, 110592\n"
      "buffer_load_dwordx4 v5, s[44:47], s38 offen lds\n"
      "s_add_u32 m0, s36, 114688\n"
      "s_mov_b32 s38, 114688\n"
      "buffer_load_dwordx4 v5, s[44:47], s38 offen lds\n"
      "s_add_u32 m0, s36, 118784\n"
      "s_mov_b32 s38, 118784\n"
      "buffer_load_dwordx4 v5, s[44:47], s38 offen lds\n"
      "s_add_u32 m0, s36, 122880\n"
      "s_mov_b32 s38, 122880\n"
      "buffer_load_dwordx4 v5, s[44:47], s38 offen lds\n"
      "s_add_u32 m0, s36, 126976\n"
      "s_mov_b32 s38, 126976\n"
      "buffer_load_dwordx4 v5, s[44:47], s38 offen lds\n"
      "s_add_u32 m0, s36, 131072\n"
      "s_mov_b32 s38, 131072\n"
      "buffer_load_dwordx4 v5, s[44:47], s38 offen lds\n"
      "s_add_u32 m0, s36, 135168\n"
      "s_mov_b32 s38, 135168\n"
      "buffer_load_dwordx4 v5, s[44:47], s38 offen lds\n"
      "s_add_u32 m0, s36, 139264\n"
      "s_mov_b32 s38, 139264\n"
      "buffer_load_dwordx4 v5, s[44:47], s38 offen lds\n"
      "s_add_u32 m0, s36, 143360\n"
      "s_mov_b32 s38, 143360\n"
      "buffer_load_dwordx4 v5, s[44:47], s38 offen lds\n"
      "s_waitcnt vmcnt(27)\n"
      "s_barrier\n"
      "v_add_u32 v20, 0, v9\n"
      "v_add_u32 v21, 0, v10\n"
      "ds_read_b64_tr_b16 a[144:145], v20 offset:0\n"
      "ds_read_b64_tr_b16 a[146:147], v20 offset:18432\n"
      "ds_read_b64_tr_b16 a[160:161], v20 offset:8\n"
      "ds_read_b64_tr_b16 a[162:163], v20 offset:18440\n"
      "ds_read_b64_tr_b16 a[176:177], v20 offset:64\n"
      "ds_read_b64_tr_b16 a[178:179], v20 offset:18496\n"
      "ds_read_b64_tr_b16 a[192:193], v20 offset:72\n"
      "ds_read_b64_tr_b16 a[194:195], v20 offset:18504\n"
      "ds_read_b64_tr_b16 a[208:209], v20 offset:128\n"
      "ds_read_b64_tr_b16 a[210:211], v20 offset:18560\n"
      "s_waitcnt lgkmcnt(0)\n"
      "ds_read_b64_tr_b16 a[224:225], v20 offset:136\n"
      "ds_read_b64_tr_b16 a[226:227], v20 offset:18568\n"
      "ds_read_b64_tr_b16 a[240:241], v20 offset:192\n"
      "ds_read_b64_tr_b16 a[242:243], v20 offset:18624\n"
      "ds_read_b64_tr_b16 v[224:225], v20 offset:200\n"
      "ds_read_b64_tr_b16 v[226:227], v20 offset:18632\n"
      "ds_read_b64_tr_b16 v[240:241], v21 offset:0\n"
      "ds_read_b64_tr_b16 v[242:243], v21 offset:18432\n"
      "s_waitcnt lgkmcnt(0)\n"
      "s_waitcnt vmcnt(18)\n"
      "s_barrier\n"
      "v_add_u32 v20, 36864, v9\n"
      "v_add_u32 v21, 36864, v10\n"
      "ds_read_b64_tr_b16 a[148:149], v20 offset:0\n"
      "ds_read_b64_tr_b16 a[150:151], v20 offset:18432\n"
      "ds_read_b64_tr_b16 a[164:165], v20 offset:8\n"
      "ds_read_b64_tr_b16 a[166:167], v20 offset:18440\n"
      "ds_read_b64_tr_b16 a[180:181], v20 offset:64\n"
      "ds_read_b64_tr_b16 a[182:183], v20 offset:18496\n"
      "ds_read_b64_tr_b16 a[196:197], v20 offset:72\n"
      "ds_read_b64_tr_b16 a[198:199], v20 offset:18504\n"
      "ds_read_b64_tr_b16 a[212:213], v20 offset:128\n"
      "ds_read_b64_tr_b16 a[214:215], v20 offset:18560\n"
      "s_waitcnt lgkmcnt(0)\n"
      "ds_read_b64_tr_b16 a[228:229], v20 offset:136\n"
      "ds_read_b64_tr_b16 a[230:231], v20 offset:18568\n"
      "ds_read_b64_tr_b16 a[244:245], v20 offset:192\n"
      "ds_read_b64_tr_b16 a[246:247], v20 offset:18624\n"
      "ds_read_b64_tr_b16 v[228:229], v20 offset:200\n"
      "ds_read_b64_tr_b16 v[230:231], v20 offset:18632\n"
      "ds_read_b64_tr_b16 v[244:245], v21 offset:0\n"
      "ds_read_b64_tr_b16 v[246:247], v21 offset:18432\n"
      "s_waitcnt lgkmcnt(0)\n"
      "s_waitcnt vmcnt(9)\n"
      "s_barrier\n"
      "v_add_u32 v20, 73728, v9\n"
      "v_add_u32 v21, 73728, v10\n"
      "ds_read_b64_tr_b16 a[152:153], v20 offset:0\n"
      "ds_read_b64_tr_b16 a[154:155], v20 offset:18432\n"
      "ds_read_b64_tr_b16 a[168:169], v20 offset:8\n"
      "ds_read_b64_tr_b16 a[170:171], v20 offset:18440\n"
      "ds_read_b64_tr_b16 a[184:185], v20 offset:64\n"
      "ds_read_b64_tr_b16 a[186:187], v20 offset:18496\n"
      "ds_read_b64_tr_b16 a[200:201], v20 offset:72\n"
      "ds_read_b64_tr_b16 a[202:203], v20 offset:18504\n"
      "ds_read_b64_tr_b16 a[216:217], v20 offset:128\n"
      "ds_read_b64_tr_b16 a[218:219], v20 offset:18560\n"
      "s_waitcnt lgkmcnt(0)\n"
      "ds_read_b64_tr_b16 a[232:233], v20 offset:136\n"
      "ds_read_b64_tr_b16 a[234:235], v20 offset:18568\n"
      "ds_read_b64_tr_b16 a[248:249], v20 offset:192\n"
      "ds_read_b64_tr_b16 a[250:251], v20 offset:18624\n"
      "ds_read_b64_tr_b16 v[232:233], v20 offset:200\n"
      "ds_read_b64_tr_b16 v[234:235], v20 offset:18632\n"
      "ds_read_b64_tr_b16 v[248:249], v21 offset:0\n"
      "ds_read_b64_tr_b16 v[250:251], v21 offset:18432\n"
      "s_waitcnt lgkmcnt(0)\n"
      "s_waitcnt vmcnt(0)\n"
      "s_barrier\n"
      "v_add_u32 v20, 110592, v9\n"
      "v_add_u32 v21, 110592, v10\n"
      "ds_read_b64_tr_b16 a[156:157], v20 offset:0\n"
      "ds_read_b64_tr_b16 a[158:159], v20 offset:18432\n"
      "ds_read_b64_tr_b16 a[172:173], v20 offset:8\n"
      "ds_read_b64_tr_b16 a[174:175], v20 offset:18440\n"
      "ds_read_b64_tr_b16 a[188:189], v20 offset:64\n"
      "ds_read_b64_tr_b16 a[190:191], v20 offset:18496\n"
      "ds_read_b64_tr_b16 a[204:205], v20 offset:72\n"
      "ds_read_b64_tr_b16 a[206:207], v20 offset:18504\n"
      "ds_read_b64_tr_b16 a[220:221], v20 offset:128\n"
      "ds_read_b64_tr_b16 a[222:223], v20 offset:18560\n"
      "s_waitcnt lgkmcnt(0)\n"
      "ds_read_b64_tr_b16 a[236:237], v20 offset:136\n"
      "ds_read_b64_tr_b16 a[238:239], v20 offset:18568\n"
      "ds_read_b64_tr_b16 a[252:253], v20 offset:192\n"
      "ds_read_b64_tr_b16 a[254:255], v20 offset:18624\n"
      "ds_read_b64_tr_b16 v[236:237], v20 offset:200\n"
      "ds_read_b64_tr_b16 v[238:239], v20 offset:18632\n"
      "ds_read_b64_tr_b16 v[252:253], v21 offset:0\n"
      "ds_read_b64_tr_b16 v[254:255], v21 offset:18432\n"
      "s_waitcnt lgkmcnt(0)\n"
      "v_mov_b32 v24, 0x3f803f80\n"
      "v_mov_b32 v25, 0x3f803f80\n"
      "v_mov_b32 v26, 0x3f803f80\n"
      "v_mov_b32 v27, 0x3f803f80\n"
      "v_mov_b32 v28, 0x3f003f00\n"
      "v_mov_b32 v29, 0x3f003f00\n"
      "v_mov_b32 v30, 0x3f003f00\n"
      "v_mov_b32 v31, 0x3f003f00\n"
      "v_mov_b32 v152, 0\n"
      "v_mov_b32 v153, 0\n"
      "v_mov_b32 v154, 0\n"
      "v_mov_b32 v155, 0\n"
      "v_mov_b32 v156, 0\n"
      "v_mov_b32 v157, 0\n"
      "v_mov_b32 v19, 0xd028965f\n"
      "v_cmp_le_i32 s[64:65], 0, v13\n"
      "v_cmp_le_i32 s[66:67], 1, v13\n"
      "v_cmp_le_i32 s[68:69], 2, v13\n"
      "v_cmp_le_i32 s[70:71], 3, v13\n"
      "v_cmp_le_i32 s[72:73], 16, v13\n"
      "v_cmp_le_i32 s[74:75], 17, v13\n"
      "v_cmp_le_i32 s[76:77], 18, v13\n"
      "v_cmp_le_i32 s[78:79], 19, v13\n"
      "s_mov_b32 s80, 0x3e0293ee\n"
      "s_mov_b32 s81, s80\n"
      "s_and_b32 s62, s52, 0xff\n"
      "s_lshr_b32 s63, s52, 8\n"
      "s_lshl_b32 s53, s63, 10\n"
      "s_add_u32 s53, s53, 2097152\n"
      "s_mov_b32 s54, 0\n"
      "s_mov_b32 s55, 0\n"
      "s_lshl_b32 s56, s62, 14\n"
      "s_add_u32 s57, s56, 134217728\n"
      "s_add_u32 s56, s56, 67108864\n"
      "v_readfirstlane_b32 s60, v14\n"
      "s_nop 3\n"
      "s_and_b32 s88, s60, 0xffff\n"
      "s_lshl_b32 s88, s88, 16\n"
      "s_and_b32 s60, s60, 0xffff0000\n"
      "s_lshr_b32 s61, s36, 2\n"
      "s_mov_b32 s37, 0\n"
      "s_cmp_lg_u32 s36, 0\n"
      "s_cbranch_scc1 lw52_%=\n"
      "s_mov_b32 s39, s98\n"
      "wf53_%=:\n"
      "buffer_load_dword v20, v7, s[40:43], 0 offen sc1\n"
      "s_waitcnt vmcnt(0)\n"
      "v_cmp_gt_i32 vcc, 0, v20\n"
      "s_cbranch_vccz wd54_%=\n"
      "s_sleep 8\n"
      "s_add_u32 s39, s39, 1\n"
      "s_cmp_lt_u32 s39, 524288\n"
      "s_cbranch_scc1 wf53_%=\n"
      "s_mov_b32 s98, 524288\n"
      "wd54_%=:\n"
      "lw52_%=:\n"
      "v_mov_b32 v21, 40960\n"
      "s_mov_b32 s39, s98\n"
      "s_mov_b64 exec, 1\n"
      "wf55_%=:\n"
      "buffer_load_dword v20, v21, s[40:43], 0 offen sc1\n"
      "s_waitcnt vmcnt(0)\n"
      "v_cmp_gt_i32 vcc, 0, v20\n"
      "s_cbranch_vccz wd56_%=\n"
      "s_sleep 4\n"
      "s_add_u32 s39, s39, 1\n"
      "s_cmp_lt_u32 s39, 524288\n"
      "s_cbranch_scc1 wf55_%=\n"
      "s_mov_b32 s98, 524288\n"
      "wd56_%=:\n"
      "s_mov_b64 exec, -1\n"
      "v_readfirstlane_b32 s38, v20\n"
      "s_nop 3\n"
      "s_and_b32 s90, s38, 8192\n"
      "s_xor_b32 s91, s90, 8192\n"
      "v_add_u32 v15, s90, v15\n"
      "s_lshr_b32 s39, s56, 8\n"
      "s_xor_b32 s39, s39, s90\n"
      "s_and_b32 s39, s39, 8192\n"
      "s_bitset0_b32 s56, 13\n"
      "s_or_b32 s56, s56, s39\n"
      "s_lshr_b32 s39, s57, 8\n"
      "s_xor_b32 s39, s39, s90\n"
      "s_and_b32 s39, s39, 8192\n"
      "s_bitset0_b32 s57, 13\n"
      "s_or_b32 s57, s57, s39\n"
      "s_barrier\n"
      "s_mov_b32 s39, s98\n"
      "s_mov_b64 exec, 4\n"
      "wf57_%=:\n"
      "buffer_load_dword v20, v15, s[40:43], 0 offen sc1\n"
      "s_waitcnt vmcnt(0)\n"
      "v_cmp_gt_i32 vcc, 1, v20\n"
      "s_cbranch_vccz wd58_%=\n"
      "s_sleep 4\n"
      "s_add_u32 s39, s39, 1\n"
      "s_cmp_lt_u32 s39, 524288\n"
      "s_cbranch_scc1 wf57_%=\n"
      "s_mov_b32 s98, 524288\n"
      "wd58_%=:\n"
      "s_mov_b64 exec, -1\n"
      "s_mov_b32 s58, s36\n"
      "s_mov_b32 s59, s61\n"
      "s_mov_b32 s89, s56\n"
      "s_add_u32 m0, s58, 18432\n"
      "s_add_u32 s38, s89, 0\n"
      "buffer_load_dwordx4 v5, s[40:43], s38 offen sc1 lds\n"
      "s_add_u32 m0, s58, 22528\n"
      "s_add_u32 s38, s89, 4096\n"
      "buffer_load_dwordx4 v5, s[40:43], s38 offen sc1 lds\n"
      "s_add_u32 m0, s58, 0\n"
      "s_add_u32 s38, s53, 0\n"
      "buffer_load_dwordx4 v16, s[40:43], s38 offen sc1 lds\n"
      "s_add_u32 m0, s58, 4096\n"
      "s_add_u32 s38, s53, 8192\n"
      "buffer_load_dwordx4 v16, s[40:43], s38 offen sc1 lds\n"
      "s_add_u32 m0, s58, 8192\n"
      "s_add_u32 s38, s53, 16384\n"
      "buffer_load_dwordx4 v16, s[40:43], s38 offen sc1 lds\n"
      "s_add_u32 m0, s58, 12288\n"
      "s_add_u32 s38, s53, 24576\n"
      "buffer_load_dwordx4 v16, s[40:43], s38 offen sc1 lds\n"
      "s_add_u32 m0, s59, 16384\n"
      "s_add_u32 s38, s53, 32768\n"
      "buffer_load_dword v17, s[40:43], s38 offen sc1 lds\n"
      "s_add_u32 m0, s59, 17408\n"
      "s_add_u32 s38, s53, 34816\n"
      "buffer_load_dword v17, s[40:43], s38 offen sc1 lds\n"
      "s_mov_b32 s82, -1\n"
      "s_mov_b32 s86, -1\n"
      "v_mov_b32 v18, v6\n"
      "s_waitcnt vmcnt(0)\n"
      "s_barrier\n"
      "ds_read_b128 v[32:35], v18 offset:0\n"
      "ds_read_b128 v[36:39], v18 offset:1024\n"
      "ds_read_b128 v[40:43], v18 offset:2048\n"
      "ds_read_b128 v[44:47], v18 offset:3072\n"
      "ds_read_b128 v[108:111], v18 offset:4096\n"
      "s_add_u32 s37, s37, 1\n"
      "s_add_u32 s87, s37, 1\n"
      "s_min_u32 s87, s87, 64\n"
      "s_mov_b32 s56, s89\n"
      "s_add_u32 s89, s89, 1048576\n"
      "s_lshr_b32 s39, s89, 8\n"
      "s_xor_b32 s39, s39, s90\n"
      "s_and_b32 s39, s39, 8192\n"
      "s_bitset0_b32 s89, 13\n"
      "s_or_b32 s89, s89, s39\n"
      "s_add_u32 s53, s53, 36864\n"
      "s_xor_b32 s54, s54, 26624\n"
      "s_add_u32 s58, s54, s36\n"
      "s_add_u32 s59, s54, s61\n"
      "qotop59_%=:\n"
      "s_cmp_lt_i32 s86, s87\n"
      "s_cbranch_scc1 qoslow61_%=\n"
      "qogo60_%=:\n"
      "s_cmp_lt_i32 s82, s37\n"
      "s_cbranch_scc1 qolate62_%=\n"
      "s_add_u32 m0, s36, 53248\n"
      "s_nop 0\n"
      "buffer_load_dwordx4 v5, s[40:43], s57 offen sc1 lds\n"
      "s_add_u32 s38, s57, 4096\n"
      "buffer_load_dwordx4 v[132:135], v5, s[40:43], s38 offen sc1\n"
      "qolate62_%=:\n"
      "s_waitcnt lgkmcnt(4)\n"
      "v_mfma_f32_16x16x32_bf16 v[48:51], a[0:3], v[32:35], 0\n"
      "ds_read_b128 v[112:115], v18 offset:5120\n"
      "v_mfma_f32_16x16x32_bf16 v[52:55], a[72:75], v[32:35], 0\n"
      "s_add_u32 s92, s60, 0\n"
      "s_lshr_b32 s97, s92, 8\n"
      "s_xor_b32 s97, s97, s91\n"
      "s_waitcnt lgkmcnt(4)\n"
      "v_mfma_f32_16x16x32_bf16 v[48:51], a[4:7], v[36:39], v[48:51]\n"
      "ds_read_b128 v[32:35], v18 offset:6144\n"
      "v_mfma_f32_16x16x32_bf16 v[52:55], a[76:79], v[36:39], v[52:55]\n"
      "s_and_b32 s97, s97, 8192\n"
      "s_or_b32 s92, s92, s97\n"
      "s_waitcnt lgkmcnt(4)\n"
      "v_mfma_f32_16x16x32_bf16 v[48:51], a[8:11], v[40:43], v[48:51]\n"
      "ds_read_b128 v[36:39], v18 offset:7168\n"
      "v_mfma_f32_16x16x32_bf16 v[52:55], a[80:83], v[40:43], v[52:55]\n"
      "s_add_u32 s93, s60, 65536\n"
      "s_lshr_b32 s97, s93, 8\n"
      "s_xor_b32 s97, s97, s91\n"
      "s_waitcnt lgkmcnt(4)\n"
      "v_mfma_f32_16x16x32_bf16 v[48:51], a[12:15], v[44:47], v[48:51]\n"
      "ds_read_b128 v[40:43], v18 offset:8192\n"
      "v_mfma_f32_16x16x32_bf16 v[52:55], a[84:87], v[44:47], v[52:55]\n"
      "s_add_u32 s38, s56, 524288\n"
      "buffer_load_dwordx4 v[124:127], v5, s[40:43], s38 offen sc1\n"
      "s_waitcnt lgkmcnt(4)\n"
      "v_mfma_f32_16x16x32_bf16 v[48:51], a[16:19], v[108:111], v[48:51]\n"
      "ds_read_b128 v[44:47], v18 offset:9216\n"
      "v_mfma_f32_16x16x32_bf16 v[52:55], a[88:91], v[108:111], v[52:55]\n"
      "s_and_b32 s97, s97, 8192\n"
      "s_or_b32 s93, s93, s97\n"
      "s_waitcnt lgkmcnt(4)\n"
      "v_mfma_f32_16x16x32_bf16 v[48:51], a[20:23], v[112:115], v[48:51]\n"
      "ds_read_b128 v[108:111], v18 offset:10240\n"
      "v_mfma_f32_16x16x32_bf16 v[52:55], a[92:95], v[112:115], v[52:55]\n"
      "s_add_u32 s94, s60, 131072\n"
      "s_lshr_b32 s97, s94, 8\n"
      "s_xor_b32 s97, s97, s91\n"
      "s_waitcnt lgkmcnt(4)\n"
      "v_mfma_f32_16x16x32_bf16 v[48:51], a[24:27], v[32:35], v[48:51]\n"
      "ds_read_b128 v[112:115], v18 offset:11264\n"
      "v_mfma_f32_16x16x32_bf16 v[52:55], a[96:99], v[32:35], v[52:55]\n"
      "s_add_u32 s38, s56, 528384\n"
      "buffer_load_dwordx4 v[128:131], v5, s[40:43], s38 offen sc1\n"
      "s_waitcnt lgkmcnt(4)\n"
      "v_mfma_f32_16x16x32_bf16 v[48:51], a[28:31], v[36:39], v[48:51]\n"
      "ds_read_b128 v[32:35], v18 offset:12288\n"
      "v_mfma_f32_16x16x32_bf16 v[52:55], a[100:103], v[36:39], v[52:55]\n"
      "s_and_b32 s97, s97, 8192\n"
      "s_or_b32 s94, s94, s97\n"
      "s_waitcnt lgkmcnt(4)\n"
      "v_mfma_f32_16x16x32_bf16 v[48:51], a[32:35], v[40:43], v[48:51]\n"
      "ds_read_b128 v[36:39], v18 offset:13312\n"
      "v_mfma_f32_16x16x32_bf16 v[52:55], a[104:107], v[40:43], v[52:55]\n"
      "s_add_u32 s95, s60, 196608\n"
      "s_lshr_b32 s97, s95, 8\n"
      "s_xor_b32 s97, s97, s91\n"
      "s_waitcnt lgkmcnt(4)\n"
      "v_mfma_f32_16x16x32_bf16 v[48:51], a[36:39], v[44:47], v[48:51]\n"
      "ds_read_b128 v[40:43], v18 offset:14336\n"
      "v_mfma_f32_16x16x32_bf16 v[52:55], a[108:111], v[44:47], v[52:55]\n"
      "s_add_u32 m0, s58, 18432\n"
      "s_add_u32 s38, s89, 0\n"
      "buffer_load_dwordx4 v5, s[40:43], s38 offen sc1 lds\n"
      "s_waitcnt lgkmcnt(4)\n"
      "v_mfma_f32_16x16x32_bf16 v[48:51], a[40:43], v[108:111], v[48:51]\n"
      "ds_read_b128 v[44:47], v18 offset:15360\n"
      "ds_read_b128 v[76:79], v18 offset:18432\n"
      "v_mfma_f32_16x16x32_bf16 v[52:55], a[112:115], v[108:111], v[52:55]\n"
      "s_and_b32 s97, s97, 8192\n"
      "s_or_b32 s95, s95, s97\n"
      "s_waitcnt lgkmcnt(5)\n"
      "v_mfma_f32_16x16x32_bf16 v[48:51], a[44:47], v[112:115], v[48:51]\n"
      "ds_read_b128 v[108:111], v18 offset:16384\n"
      "ds_read_b128 v[80:83], v18 offset:19456\n"
      "v_mfma_f32_16x16x32_bf16 v[52:55], a[116:119], v[112:115], v[52:55]\n"
      "s_mov_b32 s96, s88\n"
      "s_lshr_b32 s97, s96, 8\n"
      "s_xor_b32 s97, s97, s91\n"
      "s_waitcnt lgkmcnt(6)\n"
      "v_mfma_f32_16x16x32_bf16 v[48:51], a[48:51], v[32:35], v[48:51]\n"
      "ds_read_b128 v[112:115], v18 offset:17408\n"
      "ds_read_b128 v[84:87], v18 offset:20480\n"
      "v_mfma_f32_16x16x32_bf16 v[52:55], a[120:123], v[32:35], v[52:55]\n"
      "s_add_u32 m0, s58, 22528\n"
      "s_add_u32 s38, s89, 4096\n"
      "buffer_load_dwordx4 v5, s[40:43], s38 offen sc1 lds\n"
      "s_waitcnt lgkmcnt(7)\n"
      "v_mfma_f32_16x16x32_bf16 v[48:51], a[52:55], v[36:39], v[48:51]\n"
      "ds_read_b128 v[88:91], v18 offset:21504\n"
      "v_mfma_f32_16x16x32_bf16 v[52:55], a[124:127], v[36:39], v[52:55]\n"
      "s_and_b32 s97, s97, 8192\n"
      "s_or_b32 s96, s96, s97\n"
      "s_waitcnt lgkmcnt(5)\n"
      "v_mfma_f32_16x16x32_bf16 v[48:51], a[56:59], v[40:43], v[48:51]\n"
      "ds_read_b128 v[92:95], v18 offset:22528\n"
      "v_mfma_f32_16x16x32_bf16 v[52:55], a[128:131], v[40:43], v[52:55]\n"
      "s_waitcnt lgkmcnt(5)\n"
      "v_mfma_f32_16x16x32_bf16 v[48:51], a[60:63], v[44:47], v[48:51]\n"
      "ds_read_b128 v[96:99], v18 offset:23552\n"
      "v_mfma_f32_16x16x32_bf16 v[52:55], a[132:135], v[44:47], v[52:55]\n"
      "s_add_u32 m0, s58, 0\n"
      "s_add_u32 s38, s53, 0\n"
      "buffer_load_dwordx4 v16, s[40:43], s38 offen sc1 lds\n"
      "s_waitcnt lgkmcnt(5)\n"
      "v_mfma_f32_16x16x32_bf16 v[48:51], a[64:67], v[108:111], v[48:51]\n"
      "ds_read_b128 v[100:103], v18 offset:24576\n"
      "v_mfma_f32_16x16x32_bf16 v[52:55], a[136:139], v[108:111], v[52:55]\n"
      "s_waitcnt lgkmcnt(5)\n"
      "v_mfma_f32_16x16x32_bf16 v[48:51], a[68:71], v[112:115], v[48:51]\n"
      "ds_read_b128 v[104:107], v18 offset:25600\n"
      "v_mfma_f32_16x16x32_bf16 v[52:55], a[140:143], v[112:115], v[52:55]\n"
      "s_add_u32 m0, s58, 4096\n"
      "s_add_u32 s38, s53, 8192\n"
      "buffer_load_dwordx4 v16, s[40:43], s38 offen sc1 lds\n"
      "s_sub_u32 s83, s37, 1\n"
      "v_mov_b32 v22, s83\n"
      "v_cvt_pk_bf16_f32 v56, v48, v49\n"
      "v_cvt_pk_bf16_f32 v57, v50, v51\n"
      "s_nop 0\n"
      "v_cvt_pk_bf16_f32 v58, v52, v53\n"
      "v_cvt_pk_bf16_f32 v59, v54, v55\n"
      "ds_write_b128 v5, v[56:59] offset:57344\n"
      "s_add_u32 m0, s58, 8192\n"
      "s_add_u32 s38, s53, 16384\n"
      "buffer_load_dwordx4 v16, s[40:43], s38 offen sc1 lds\n"
      "s_waitcnt vmcnt(7) lgkmcnt(0)\n"
      "s_barrier\n"
      "ds_read_b128 v[60:63], v6 offset:57344\n"
      "ds_read_b128 v[64:67], v6 offset:58368\n"
      "ds_read_b128 v[68:71], v6 offset:59392\n"
      "ds_read_b128 v[72:75], v6 offset:60416\n"
      "s_add_u32 m0, s58, 12288\n"
      "s_add_u32 s38, s53, 24576\n"
      "buffer_load_dwordx4 v16, s[40:43], s38 offen sc1 lds\n"
      "s_waitcnt lgkmcnt(3)\n"
      "v_mfma_f32_16x16x32_bf16 v[136:139], v[76:79], v[60:63], 0\n"
      "v_mfma_f32_16x16x32_bf16 v[140:143], v[80:83], v[60:63], 0\n"
      "s_waitcnt lgkmcnt(2)\n"
      "v_mfma_f32_16x16x32_bf16 v[136:139], v[84:87], v[64:67], v[136:139]\n"
      "s_add_u32 m0, s59, 16384\n"
      "s_add_u32 s38, s53, 32768\n"
      "buffer_load_dword v17, s[40:43], s38 offen sc1 lds\n"
      "v_mfma_f32_16x16x32_bf16 v[140:143], v[88:91], v[64:67], v[140:143]\n"
      "s_waitcnt lgkmcnt(1)\n"
      "v_mfma_f32_16x16x32_bf16 v[136:139], v[92:95], v[68:71], v[136:139]\n"
      "s_add_u32 m0, s59, 17408\n"
      "s_add_u32 s38, s53, 34816\n"
      "buffer_load_dword v17, s[40:43], s38 offen sc1 lds\n"
      "v_mfma_f32_16x16x32_bf16 v[140:143], v[96:99], v[68:71], v[140:143]\n"
      "s_waitcnt lgkmcnt(0)\n"
      "v_mfma_f32_16x16x32_bf16 v[136:139], v[100:103], v[72:75], v[136:139]\n"
      "v_mfma_f32_16x16x32_bf16 v[140:143], v[104:107], v[72:75], v[140:143]\n"
      "buffer_store_dword v22, v8, s[40:43], 0 offen sc1\n"
      "s_nop 5\n"
      "v_cndmask_b32 v136, v19, v136, s[64:65]\n"
      "v_cndmask_b32 v137, v19, v137, s[66:67]\n"
      "v_cndmask_b32 v138, v19, v138, s[68:69]\n"
      "v_cndmask_b32 v139, v19, v139, s[70:71]\n"
      "v_cndmask_b32 v140, v19, v140, s[72:73]\n"
      "v_cndmask_b32 v141, v19, v141, s[74:75]\n"
      "v_cndmask_b32 v142, v19, v142, s[76:77]\n"
      "v_cndmask_b32 v143, v19, v143, s[78:79]\n"
      "v_max3_f32 v158, v136, v137, v138\n"
      "v_max3_f32 v159, v139, v140, v141\n"
      "v_max3_f32 v158, v158, v142, v143\n"
      "v_max_f32 v158, v158, v159\n"
      "s_cmp_ge_i32 s82, s37\n"
      "s_cbranch_scc1 qohave63_%=\n"
      "s_mov_b32 s39, s98\n"
      "s_mov_b64 exec, 3\n"
      "wf65_%=:\n"
      "buffer_load_dword v20, v15, s[40:43], 0 offen sc1\n"
      "s_waitcnt vmcnt(0)\n"
      "v_cmp_gt_i32 vcc, s37, v20\n"
      "s_cbranch_vccz wd66_%=\n"
      "s_sleep 2\n"
      "s_add_u32 s39, s39, 1\n"
      "s_cmp_lt_u32 s39, 524288\n"
      "s_cbranch_scc1 wf65_%=\n"
      "s_mov_b32 s98, 524288\n"
      "wd66_%=:\n"
      "s_mov_b64 exec, -1\n"
      "s_add_u32 m0, s36, 53248\n"
      "s_nop 0\n"
      "buffer_load_dwordx4 v5, s[40:43], s57 offen sc1 lds\n"
      "s_add_u32 s38, s57, 4096\n"
      "buffer_load_dwordx4 v[132:135], v5, s[40:43], s38 offen sc1\n"
      "s_waitcnt vmcnt(0)\n"
      "qohave63_%=:\n"
      "s_barrier\n"
      "ds_read_b128 v[108:111], v6 offset:53248\n"
      "ds_read_b128 v[112:115], v6 offset:54272\n"
      "ds_read_b128 v[116:119], v6 offset:55296\n"
      "ds_read_b128 v[120:123], v6 offset:56320\n"
      "s_add_u32 s57, s57, 524288\n"
      "s_lshr_b32 s39, s57, 8\n"
      "s_xor_b32 s39, s39, s90\n"
      "s_and_b32 s39, s39, 8192\n"
      "s_bitset0_b32 s57, 13\n"
      "s_or_b32 s57, s57, s39\n"
      "s_waitcnt lgkmcnt(3)\n"
      "v_mfma_f32_16x16x32_bf16 v[144:147], v[108:111], v[60:63], 0\n"
      "s_waitcnt lgkmcnt(2)\n"
      "v_mfma_f32_16x16x32_bf16 v[144:147], v[112:115], v[64:67], v[144:147]\n"
      "s_waitcnt lgkmcnt(1)\n"
      "v_mfma_f32_16x16x32_bf16 v[144:147], v[116:119], v[68:71], v[144:147]\n"
      "s_waitcnt lgkmcnt(0)\n"
      "v_mfma_f32_16x16x32_bf16 v[144:147], v[120:123], v[72:75], v[144:147]\n"
      "s_nop 7\n"
      "v_max3_f32 v159, v158, v146, v147\n"
      "v_max3_f32 v158, v158, v146, v147\n"
      "s_nop 1\n"
      "v_permlane32_swap_b32 v158, v159\n"
      "v_max_f32 v23, v158, v159\n"
      "v_max_f32 v21, v158, v159\n"
      "s_nop 1\n"
      "v_permlane16_swap_b32 v23, v21\n"
      "v_max_f32 v158, v23, v21\n"
      "v_mul_f32 v158, s80, v158\n"
      "v_pk_fma_f32 v[136:137], v[136:137], s[80:81], v[158:159] op_sel_hi:[1,1,0] neg_lo:[0,0,1] neg_hi:[0,0,1]\n"
      "v_pk_fma_f32 v[138:139], v[138:139], s[80:81], v[158:159] op_sel_hi:[1,1,0] neg_lo:[0,0,1] neg_hi:[0,0,1]\n"
      "v_pk_fma_f32 v[140:141], v[140:141], s[80:81], v[158:159] op_sel_hi:[1,1,0] neg_lo:[0,0,1] neg_hi:[0,0,1]\n"
      "v_pk_fma_f32 v[142:143], v[142:143], s[80:81], v[158:159] op_sel_hi:[1,1,0] neg_lo:[0,0,1] neg_hi:[0,0,1]\n"
      "v_pk_fma_f32 v[146:147], v[146:147], s[80:81], v[158:159] op_sel_hi:[1,1,0] neg_lo:[0,0,1] neg_hi:[0,0,1]\n"
      "v_exp_f32 v136, v136\n"
      "v_exp_f32 v137, v137\n"
      "v_exp_f32 v138, v138\n"
      "v_exp_f32 v139, v139\n"
      "v_exp_f32 v140, v140\n"
      "v_exp_f32 v141, v141\n"
      "v_exp_f32 v142, v142\n"
      "v_exp_f32 v143, v143\n"
      "v_cvt_pk_bf16_f32 v148, v136, v137\n"
      "v_cvt_pk_bf16_f32 v149, v138, v139\n"
      "v_cvt_pk_bf16_f32 v150, v140, v141\n"
      "v_cvt_pk_bf16_f32 v151, v142, v143\n"
      "v_exp_f32 v146, v146\n"
      "v_exp_f32 v147, v147\n"
      "v_mfma_f32_16x16x32_bf16 v[48:51], v[24:27], v[148:151], 0\n"
      "v_cvt_pk_bf16_f32 v154, v146, v147\n"
      "v_cvt_pk_bf16_f32 v155, v146, v147\n"
      "s_waitcnt vmcnt(9)\n"
      "v_mfma_f32_16x16x32_bf16 v[160:163], v[124:127], v[148:151], 0\n"
      "v_mfma_f32_16x16x32_bf16 v[48:51], v[28:31], v[152:155], v[48:51]\n"
      "v_mfma_f32_16x16x32_bf16 v[160:163], v[132:135], v[154:157], v[160:163]\n"
      "v_mfma_f32_16x16x32_bf16 v[164:167], v[128:131], v[148:151], 0\n"
      "v_rcp_f32 v158, v48\n"
      "v_mfma_f32_16x16x32_bf16 v[164:167], v[132:135], v[152:155], v[164:167]\n"
      "v_mul_f32 v160, v160, v158\n"
      "v_mul_f32 v161, v161, v158\n"
      "v_mul_f32 v162, v162, v158\n"
      "v_mul_f32 v163, v163, v158\n"
      "v_cvt_pk_bf16_f32 v56, v160, v161\n"
      "v_cvt_pk_bf16_f32 v57, v162, v163\n"
      "s_nop 0\n"
      "v_pk_mul_f32 v[164:165], v[164:165], v[158:159] op_sel_hi:[1,0]\n"
      "v_pk_mul_f32 v[166:167], v[166:167], v[158:159] op_sel_hi:[1,0]\n"
      "v_cvt_pk_bf16_f32 v58, v164, v165\n"
      "v_cvt_pk_bf16_f32 v59, v166, v167\n"
      "ds_write_b128 v5, v[56:59] offset:61440\n"
      "v_xor_b32 v18, 26624, v18\n"
      "s_waitcnt vmcnt(1) lgkmcnt(0)\n"
      "s_barrier\n"
      "ds_read_b128 v[60:63], v6 offset:61440\n"
      "ds_read_b128 v[64:67], v6 offset:62464\n"
      "ds_read_b128 v[68:71], v6 offset:63488\n"
      "ds_read_b128 v[72:75], v6 offset:64512\n"
      "s_mov_b64 exec, 7\n"
      "buffer_load_dword v20, v15, s[40:43], 0 offen sc1\n"
      "s_mov_b64 exec, -1\n"
      "s_waitcnt lgkmcnt(3)\n"
      "v_mfma_f32_16x16x32_bf16 v[200:203], v[240:243], v[60:63], 0\n"
      "s_waitcnt lgkmcnt(2)\n"
      "v_mfma_f32_16x16x32_bf16 v[200:203], v[244:247], v[64:67], v[200:203]\n"
      "s_waitcnt lgkmcnt(1)\n"
      "v_mfma_f32_16x16x32_bf16 v[200:203], v[248:251], v[68:71], v[200:203]\n"
      "s_waitcnt lgkmcnt(0)\n"
      "v_mfma_f32_16x16x32_bf16 v[200:203], v[252:255], v[72:75], v[200:203]\n"
      "v_mfma_f32_16x16x32_bf16 v[168:171], a[144:147], v[60:63], 0\n"
      "v_mfma_f32_16x16x32_bf16 v[168:171], a[148:151], v[64:67], v[168:171]\n"
      "v_cvt_pk_bf16_f32 v220, v200, v201\n"
      "v_cvt_pk_bf16_f32 v221, v202, v203\n"
      "v_mfma_f32_16x16x32_bf16 v[168:171], a[152:155], v[68:71], v[168:171]\n"
      "buffer_store_dwordx2 v[220:221], v12, s[40:43], s96 offen sc1\n"
      "v_mfma_f32_16x16x32_bf16 v[168:171], a[156:159], v[72:75], v[168:171]\n"
      "v_mfma_f32_16x16x32_bf16 v[172:175], a[160:163], v[60:63], 0\n"
      "v_mfma_f32_16x16x32_bf16 v[172:175], a[164:167], v[64:67], v[172:175]\n"
      "v_cvt_pk_bf16_f32 v204, v168, v169\n"
      "v_cvt_pk_bf16_f32 v205, v170, v171\n"
      "v_mfma_f32_16x16x32_bf16 v[172:175], a[168:171], v[68:71], v[172:175]\n"
      "v_mfma_f32_16x16x32_bf16 v[172:175], a[172:175], v[72:75], v[172:175]\n"
      "v_mfma_f32_16x16x32_bf16 v[176:179], a[176:179], v[60:63], 0\n"
      "v_mfma_f32_16x16x32_bf16 v[176:179], a[180:183], v[64:67], v[176:179]\n"
      "v_cvt_pk_bf16_f32 v206, v172, v173\n"
      "v_cvt_pk_bf16_f32 v207, v174, v175\n"
      "v_mfma_f32_16x16x32_bf16 v[176:179], a[184:187], v[68:71], v[176:179]\n"
      "buffer_store_dwordx4 v[204:207], v11, s[40:43], s92 offen sc1\n"
      "v_mfma_f32_16x16x32_bf16 v[176:179], a[188:191], v[72:75], v[176:179]\n"
      "v_mfma_f32_16x16x32_bf16 v[180:183], a[192:195], v[60:63], 0\n"
      "v_mfma_f32_16x16x32_bf16 v[180:183], a[196:199], v[64:67], v[180:183]\n"
      "v_cvt_pk_bf16_f32 v208, v176, v177\n"
      "v_cvt_pk_bf16_f32 v209, v178, v179\n"
      "v_mfma_f32_16x16x32_bf16 v[180:183], a[200:203], v[68:71], v[180:183]\n"
      "v_mfma_f32_16x16x32_bf16 v[180:183], a[204:207], v[72:75], v[180:183]\n"
      "v_mfma_f32_16x16x32_bf16 v[184:187], a[208:211], v[60:63], 0\n"
      "v_mfma_f32_16x16x32_bf16 v[184:187], a[212:215], v[64:67], v[184:187]\n"
      "v_cvt_pk_bf16_f32 v210, v180, v181\n"
      "v_cvt_pk_bf16_f32 v211, v182, v183\n"
      "v_mfma_f32_16x16x32_bf16 v[184:187], a[216:219], v[68:71], v[184:187]\n"
      "buffer_store_dwordx4 v[208:211], v11, s[40:43], s93 offen sc1\n"
      "v_mfma_f32_16x16x32_bf16 v[184:187], a[220:223], v[72:75], v[184:187]\n"
      "v_mfma_f32_16x16x32_bf16 v[188:191], a[224:227], v[60:63], 0\n"
      "v_mfma_f32_16x16x32_bf16 v[188:191], a[228:231], v[64:67], v[188:191]\n"
      "v_cvt_pk_bf16_f32 v212, v184, v185\n"
      "v_cvt_pk_bf16_f32 v213, v186, v187\n"
      "v_mfma_f32_16x16x32_bf16 v[188:191], a[232:235], v[68:71], v[188:191]\n"
      "v_mfma_f32_16x16x32_bf16 v[188:191], a[236:239], v[72:75], v[188:191]\n"
      "ds_read_b128 v[32:35], v18 offset:0\n"
      "v_mfma_f32_16x16x32_bf16 v[192:195], a[240:243], v[60:63], 0\n"
      "ds_read_b128 v[36:39], v18 offset:1024\n"
      "v_mfma_f32_16x16x32_bf16 v[192:195], a[244:247], v[64:67], v[192:195]\n"
      "v_cvt_pk_bf16_f32 v214, v188, v189\n"
      "v_cvt_pk_bf16_f32 v215, v190, v191\n"
      "v_mfma_f32_16x16x32_bf16 v[192:195], a[248:251], v[68:71], v[192:195]\n"
      "buffer_store_dwordx4 v[212:215], v11, s[40:43], s94 offen sc1\n"
      "v_mfma_f32_16x16x32_bf16 v[192:195], a[252:255], v[72:75], v[192:195]\n"
      "ds_read_b128 v[40:43], v18 offset:2048\n"
      "v_mfma_f32_16x16x32_bf16 v[196:199], v[224:227], v[60:63], 0\n"
      "ds_read_b128 v[44:47], v18 offset:3072\n"
      "v_mfma_f32_16x16x32_bf16 v[196:199], v[228:231], v[64:67], v[196:199]\n"
      "v_cvt_pk_bf16_f32 v216, v192, v193\n"
      "v_cvt_pk_bf16_f32 v217, v194, v195\n"
      "v_mfma_f32_16x16x32_bf16 v[196:199], v[232:235], v[68:71], v[196:199]\n"
      "ds_read_b128 v[108:111], v18 offset:4096\n"
      "v_mfma_f32_16x16x32_bf16 v[196:199], v[236:239], v[72:75], v[196:199]\n"
      "s_add_u32 s60, s60, 2359296\n"
      "s_add_u32 s88, s88, 2359296\n"
      "s_waitcnt vmcnt(4)\n"
      "v_readlane_b32 s82, v20, 0\n"
      "v_readlane_b32 s84, v20, 1\n"
      "v_readlane_b32 s86, v20, 2\n"
      "s_nop 1\n"
      "v_cvt_pk_bf16_f32 v218, v196, v197\n"
      "v_cvt_pk_bf16_f32 v219, v198, v199\n"
      "buffer_store_dwordx4 v[216:219], v11, s[40:43], s95 offen sc1\n"
      "s_min_i32 s82, s82, s84\n"
      "s_add_u32 s37, s37, 1\n"
      "s_add_u32 s87, s37, 1\n"
      "s_min_u32 s87, s87, 64\n"
      "s_mov_b32 s56, s89\n"
      "s_add_u32 s89, s89, 1048576\n"
      "s_lshr_b32 s39, s89, 8\n"
      "s_xor_b32 s39, s39, s90\n"
      "s_and_b32 s39, s39, 8192\n"
      "s_bitset0_b32 s89, 13\n"
      "s_or_b32 s89, s89, s39\n"
      "s_add_u32 s53, s53, 36864\n"
      "s_xor_b32 s54, s54, 26624\n"
      "s_add_u32 s58, s54, s36\n"
      "s_add_u32 s59, s54, s61\n"
      "s_cmp_le_u32 s37, 64\n"
      "s_cbranch_scc1 qotop59_%=\n"
      "v_mov_b32 v20, 64\n"
      "s_waitcnt vmcnt(0)\n"
      "s_barrier\n"
      "buffer_store_dword v20, v8, s[40:43], 0 offen sc1\n"
      "s_waitcnt vmcnt(0)\n"
      "s_branch qodone64_%=\n"
      "qoslow61_%=:\n"
      "s_mov_b32 s39, s98\n"
      "s_mov_b64 exec, 4\n"
      "wf67_%=:\n"
      "buffer_load_dword v20, v15, s[40:43], 0 offen sc1\n"
      "s_waitcnt vmcnt(0)\n"
      "v_cmp_gt_i32 vcc, s87, v20\n"
      "s_cbranch_vccz wd68_%=\n"
      "s_sleep 4\n"
      "s_add_u32 s39, s39, 1\n"
      "s_cmp_lt_u32 s39, 524288\n"
      "s_cbranch_scc1 wf67_%=\n"
      "s_mov_b32 s98, 524288\n"
      "wd68_%=:\n"
      "s_mov_b64 exec, -1\n"
      "s_branch qogo60_%=\n"
      "qodone64_%=:\n"
      "s_endpgm\n"
      :: "{v0}"(tid) : "memory", "v255", "a255", "s99");
    return;
  }
  const int b = slot;
  unsigned farb = 0;
  auto place = [&](unsigned a) { return a | (((a >> 8) ^ farb) & 8192u); };
  auto w2row = [&](int f) { return place(W2C + (f >> 1) * 16384 + (f & 1) * 4096); };
  auto copy_w2 = [&]() {
    const __amdgpu_buffer_rsrc_t r2 = __builtin_amdgcn_make_buffer_rsrc((void*)(p.w2 + b * 98304), 0, 196608, 0x00020000);
    v4u t1[CPB], t2[CPB];
#pragma unroll
    for (int j = 0; j < CPB; ++j) t1[j] = __builtin_amdgcn_raw_buffer_load_b128(r2, tid * 16, j * 4096, 2);
#pragma unroll 1
    for (int i = 0; i < 48; i += 2 * CPB) {
#pragma unroll
      for (int j = 0; j < CPB; ++j) t2[j] = __builtin_amdgcn_raw_buffer_load_b128(r2, tid * 16, (i + CPB + j) * 4096, 2);
      __builtin_amdgcn_sched_barrier(0);
#pragma unroll
      for (int j = 0; j < CPB; ++j) __builtin_amdgcn_raw_buffer_store_b128(t1[j], ws, tid * 16, w2row(48 * b + i + j), 16);
      __builtin_amdgcn_sched_barrier(0);
#pragma unroll
      for (int j = 0; j < CPB; ++j) t1[j] = __builtin_amdgcn_raw_buffer_load_b128(r2, tid * 16 + (i + 2 * CPB) * 4096, j * 4096, 2);
      __builtin_amdgcn_sched_barrier(0);
#pragma unroll
      for (int j = 0; j < CPB; ++j) __builtin_amdgcn_raw_buffer_store_b128(t2[j], ws, tid * 16, w2row(48 * b + i + CPB + j), 16);
      __builtin_amdgcn_sched_barrier(0);
    }
  };
  {
    const v4u ones = {~0u, ~0u, ~0u, ~0u};
    unsigned word = 0;
    if (b != 1) word = greet_post(ws, __builtin_amdgcn_raw_buffer_load_b32(ws, w == 0 ? hsa : OOB, 0, 16), tid);
    if (b == 1) {
      greet_host(ws, tid, budget);
    } else if (b == 0) {
      if (w == 0) {
        __builtin_amdgcn_raw_buffer_store_b128(ones, ws, l < 16 ? H2BASE + 8128 + (l >> 2) * 8192 + (l & 3) * 16 : OOB, 0, 16);
        unsigned sel = 0;
#pragma unroll 1
        for (int bit = 8192; bit <= 16384; bit <<= 1) {
          unsigned best = ~0u;
#pragma unroll 1
          for (int i = 0; i < 6; ++i) {
            asm volatile("s_waitcnt vmcnt(0)" ::: "memory");
            const unsigned t0 = (unsigned)__builtin_amdgcn_s_memtime();
            __builtin_amdgcn_raw_buffer_store_b128(ones, ws, l < 4 ? H2BASE + 8128 + l * 16 : OOB, sel + (i & 1) * bit, 16);
            asm volatile("s_waitcnt vmcnt(0)" ::: "memory");
            best = min(best, ((unsigned)__builtin_amdgcn_s_memtime() - t0) << 1 | (i & 1));
          }
          sel += (best & 1) * bit;
          if (bit == 8192) {
            greet_wait(ws, word, l, budget);
            __builtin_amdgcn_raw_buffer_store_b32(sel, ws, l < 3 ? (l ? G2F + (l - 1) * 128 : PARW) : OOB, 0, 16);
          }
        }
        if (l == 0) *LDSP(unsigned, 64) = sel ^ FLIP14;
      }
      __syncthreads();
      const unsigned pw = *LDSP(unsigned, 64);
      for (int ch = tid; ch < 576; ch += 256) {
        const int line = (ch >> 3) & 7, row = 2 * (line >> 1) + ((ch >> 2) & 1), cp = (ch & 3) ^ fswz(line >> 1), half = line & 1;
        const bool sh = ch >= 512;
        const int sg = ch >> 6, f = sh ? 4 : (sg < 4 ? sg : sg + 1);
        const u16* s = p.mem0 + row * 576 + 64 * f + 32 * half + 4 * cp;
        const v2u m0 = *(const v2u*)s, m1 = *(const v2u*)(s + 16);
        const v4u v = {(m0.x & 0xffffu) | m1.x << 16, m0.x >> 16 | (m1.x & 0xffff0000u), (m0.y & 0xffffu) | m1.y << 16, m0.y >> 16 | (m1.y & 0xffff0000u)};
        __builtin_amdgcn_raw_buffer_store_b128(v, ws, H2BASE + (sh ? pw ^ 16384 : pw) + (ch & 511) * 16, 0, 16);
      }
      publish(ws, PARW + 128, tid, pw);
    } else {
      for (unsigned o = ((b - 2) * 256 + tid) * 16; o < RINGS; o += 94 * 4096) __builtin_amdgcn_raw_buffer_store_b128(ones, ws, o + (o < 163840 ? H2BASE + 32768 : H1BASE - 163840), 0, 16);
      if (w == 0) greet_wait(ws, word, l, budget);
      publish(ws, G2F + b * 128, tid);
    }
  }
  if (XP & 1) return;
  {
    const int nt = b >> 2, mt = b & 3;
#pragma unroll
    for (int j = 0; j < 3; ++j) {
      const int i = (j * 4 + w) * 64 + l, row = i / 24, pos = i % 24, key = ((row >> 1) & 1) | (((row >> 3) & 1) << 1);
      TAB(TBG, j) = row * 9216 + nt * 384 + (pos >> 3) * 128 + (((pos >> 1) & 3) ^ key) * 32 + (pos & 1) * 16;
      const int ph = 3 * w + j, k2 = ((q >> 1) & 1) | ((g & 1) << 1);
      TAB(TBG, 5 + j) = (8 * g + q) * 384 + (ph >> 2) * 128 + ((ph & 3) ^ k2) * 32 + pp * 8;
    }
    TAB(TBG, 3) = g1; TAB(TBG, 4) = g3; TAB(TBG, 8) = l16;
    TAB(TBG, 9) = (192 * nt + 48 * w + 4 * g) * 2;
    const int F0 = 12 * nt + 3 * w;
    TAB(TBG, 10) = UF + (F0 >> 1) * 36864 + 9 * mt * 1024 + l * 8 + (F0 & 1) * 512;
    TAB(TBG, 11) = lo32(p.ws); TAB(TBG, 12) = hi32(p.ws); TAB(TBG, 13) = lo32(p.w1); TAB(TBG, 14) = hi32(p.w1);
    TAB(TBG, 15) = w * 1024 | mt << 24;
    if (w == 0) wait_ge(ws, LNF, 64, 0, l, budget);
    if (KGATE && w == 1) wait_ge(ws, KVP + (__builtin_amdgcn_readfirstlane(wait_ge(ws, PARW, 1, 0, l, budget)) & 8192) + h * 128, 1, KGATE - 1, l, budget);
    asm volatile("s_waitcnt vmcnt(0)" ::: "memory");
    __syncthreads();
    asm volatile("s_waitcnt lgkmcnt(0)\n"
      "v_lshlrev_b32 v4, 2, v0\n"
      "v_add_u32 v4, 147456, v4\n"
      "ds_read_b32 v5, v4 offset:0\n"
      "ds_read_b32 v6, v4 offset:1024\n"
      "ds_read_b32 v7, v4 offset:2048\n"
      "ds_read_b32 v8, v4 offset:3072\n"
      "ds_read_b32 v9, v4 offset:4096\n"
      "ds_read_b32 v10, v4 offset:5120\n"
      "ds_read_b32 v11, v4 offset:6144\n"
      "ds_read_b32 v12, v4 offset:7168\n"
      "ds_read_b32 v13, v4 offset:8192\n"
      "ds_read_b32 v14, v4 offset:9216\n"
      "ds_read_b32 v15, v4 offset:10240\n"
      "s_waitcnt lgkmcnt(0)\n"
      "ds_read_b32 v24, v4 offset:11264\n"
      "ds_read_b32 v25, v4 offset:12288\n"
      "ds_read_b32 v26, v4 offset:13312\n"
      "ds_read_b32 v27, v4 offset:14336\n"
      "ds_read_b32 v28, v4 offset:15360\n"
      "s_waitcnt lgkmcnt(0)\n"
      "v_readfirstlane_b32 s40, v24\n"
      "v_readfirstlane_b32 s41, v25\n"
      "v_readfirstlane_b32 s44, v26\n"
      "v_readfirstlane_b32 s45, v27\n"
      "v_readfirstlane_b32 s36, v28\n"
      "s_nop 4\n"
      "s_mov_b32 s42, 226492416\n"
      "s_mov_b32 s43, 0x20000\n"
      "s_mov_b32 s46, 18874368\n"
      "s_mov_b32 s47, 0x20000\n"
      "s_lshr_b32 s52, s36, 24\n"
      "s_and_b32 s36, s36, 0xffff\n"
      "s_lshr_b32 s61, s36, 2\n"
      "s_mul_i32 s53, s52, 9216\n"
      "s_add_u32 s53, s53, 4718592\n"
      "s_mov_b32 s50, 0\n"
      "s_mov_b32 s54, 0\n"
      "s_mov_b32 s55, 0\n"
      "s_mov_b32 s37, 0\n"
      "v_mov_b32 v128, 0\n"
      "v_mov_b32 v129, 0\n"
      "v_mov_b32 v130, 0\n"
      "v_mov_b32 v131, 0\n"
      "v_mov_b32 v132, 0\n"
      "v_mov_b32 v133, 0\n"
      "v_mov_b32 v134, 0\n"
      "v_mov_b32 v135, 0\n"
      "v_mov_b32 v136, 0\n"
      "v_mov_b32 v137, 0\n"
      "v_mov_b32 v138, 0\n"
      "v_mov_b32 v139, 0\n"
      "v_mov_b32 v140, 0\n"
      "v_mov_b32 v141, 0\n"
      "v_mov_b32 v142, 0\n"
      "v_mov_b32 v143, 0\n"
      "v_mov_b32 v144, 0\n"
      "v_mov_b32 v145, 0\n"
      "v_mov_b32 v146, 0\n"
      "v_mov_b32 v147, 0\n"
      "v_mov_b32 v148, 0\n"
      "v_mov_b32 v149, 0\n"
      "v_mov_b32 v150, 0\n"
      "v_mov_b32 v151, 0\n"
      "v_mov_b32 v152, 0\n"
      "v_mov_b32 v153, 0\n"
      "v_mov_b32 v154, 0\n"
      "v_mov_b32 v155, 0\n"
      "v_mov_b32 v156, 0\n"
      "v_mov_b32 v157, 0\n"
      "v_mov_b32 v158, 0\n"
      "v_mov_b32 v159, 0\n"
      "v_mov_b32 v160, 0\n"
      "v_mov_b32 v161, 0\n"
      "v_mov_b32 v162, 0\n"
      "v_mov_b32 v163, 0\n"
      "v_mov_b32 v164, 0\n"
      "v_mov_b32 v165, 0\n"
      "v_mov_b32 v166, 0\n"
      "v_mov_b32 v167, 0\n"
      "v_mov_b32 v168, 0\n"
      "v_mov_b32 v169, 0\n"
      "v_mov_b32 v170, 0\n"
      "v_mov_b32 v171, 0\n"
      "v_mov_b32 v172, 0\n"
      "v_mov_b32 v173, 0\n"
      "v_mov_b32 v174, 0\n"
      "v_mov_b32 v175, 0\n"
      "v_mov_b32 v176, 0\n"
      "v_mov_b32 v177, 0\n"
      "v_mov_b32 v178, 0\n"
      "v_mov_b32 v179, 0\n"
      "v_mov_b32 v180, 0\n"
      "v_mov_b32 v181, 0\n"
      "v_mov_b32 v182, 0\n"
      "v_mov_b32 v183, 0\n"
      "v_mov_b32 v184, 0\n"
      "v_mov_b32 v185, 0\n"
      "v_mov_b32 v186, 0\n"
      "v_mov_b32 v187, 0\n"
      "v_mov_b32 v188, 0\n"
      "v_mov_b32 v189, 0\n"
      "v_mov_b32 v190, 0\n"
      "v_mov_b32 v191, 0\n"
      "v_mov_b32 v192, 0\n"
      "v_mov_b32 v193, 0\n"
      "v_mov_b32 v194, 0\n"
      "v_mov_b32 v195, 0\n"
      "v_mov_b32 v196, 0\n"
      "v_mov_b32 v197, 0\n"
      "v_mov_b32 v198, 0\n"
      "v_mov_b32 v199, 0\n"
      "v_mov_b32 v200, 0\n"
      "v_mov_b32 v201, 0\n"
      "v_mov_b32 v202, 0\n"
      "v_mov_b32 v203, 0\n"
      "v_mov_b32 v204, 0\n"
      "v_mov_b32 v205, 0\n"
      "v_mov_b32 v206, 0\n"
      "v_mov_b32 v207, 0\n"
      "v_mov_b32 v208, 0\n"
      "v_mov_b32 v209, 0\n"
      "v_mov_b32 v210, 0\n"
      "v_mov_b32 v211, 0\n"
      "v_mov_b32 v212, 0\n"
      "v_mov_b32 v213, 0\n"
      "v_mov_b32 v214, 0\n"
      "v_mov_b32 v215, 0\n"
      "v_mov_b32 v216, 0\n"
      "v_mov_b32 v217, 0\n"
      "v_mov_b32 v218, 0\n"
      "v_mov_b32 v219, 0\n"
      "v_mov_b32 v220, 0\n"
      "v_mov_b32 v221, 0\n"
      "v_mov_b32 v222, 0\n"
      "v_mov_b32 v223, 0\n"
      "v_mov_b32 v224, 0\n"
      "v_mov_b32 v225, 0\n"
      "v_mov_b32 v226, 0\n"
      "v_mov_b32 v227, 0\n"
      "v_mov_b32 v228, 0\n"
      "v_mov_b32 v229, 0\n"
      "v_mov_b32 v230, 0\n"
      "v_mov_b32 v231, 0\n"
      "v_mov_b32 v232, 0\n"
      "v_mov_b32 v233, 0\n"
      "v_mov_b32 v234, 0\n"
      "v_mov_b32 v235, 0\n"
      "s_add_u32 s58, s54, s36\n"
      "s_add_u32 s59, s54, s61\n"
      "s_add_u32 m0, s58, 0\n"
      "s_nop 0\n"
      "buffer_load_dwordx4 v5, s[44:47], s50 offen nt lds\n"
      "s_add_u32 m0, s58, 4096\n"
      "s_nop 0\n"
      "buffer_load_dwordx4 v6, s[44:47], s50 offen nt lds\n"
      "s_add_u32 m0, s58, 8192\n"
      "s_nop 0\n"
      "buffer_load_dwordx4 v7, s[44:47], s50 offen nt lds\n"
      "s_add_u32 m0, s58, 12288\n"
      "s_add_u32 s38, s53, 0\n"
      "buffer_load_dwordx4 v8, s[40:43], s38 offen sc1 lds\n"
      "s_add_u32 m0, s58, 16384\n"
      "s_add_u32 s38, s53, 4096\n"
      "buffer_load_dwordx4 v8, s[40:43], s38 offen sc1 lds\n"
      "s_add_u32 m0, s59, 20480\n"
      "s_add_u32 s38, s53, 8192\n"
      "buffer_load_dword v9, s[40:43], s38 offen sc1 lds\n"
      "s_add_u32 s50, s50, 294912\n"
      "s_min_u32 s50, s50, 18579456\n"
      "s_add_u32 s53, s53, 36864\n"
      "s_add_u32 s54, s54, 21504\n"
      "s_cmp_eq_u32 s54, 129024\n"
      "s_cselect_b32 s54, 0, s54\n"
      "s_add_u32 s58, s54, s36\n"
      "s_add_u32 s59, s54, s61\n"
      "s_add_u32 m0, s58, 0\n"
      "s_nop 0\n"
      "buffer_load_dwordx4 v5, s[44:47], s50 offen nt lds\n"
      "s_add_u32 m0, s58, 4096\n"
      "s_nop 0\n"
      "buffer_load_dwordx4 v6, s[44:47], s50 offen nt lds\n"
      "s_add_u32 m0, s58, 8192\n"
      "s_nop 0\n"
      "buffer_load_dwordx4 v7, s[44:47], s50 offen nt lds\n"
      "s_add_u32 m0, s58, 12288\n"
      "s_add_u32 s38, s53, 0\n"
      "buffer_load_dwordx4 v8, s[40:43], s38 offen sc1 lds\n"
      "s_add_u32 m0, s58, 16384\n"
      "s_add_u32 s38, s53, 4096\n"
      "buffer_load_dwordx4 v8, s[40:43], s38 offen sc1 lds\n"
      "s_add_u32 m0, s59, 20480\n"
      "s_add_u32 s38, s53, 8192\n"
      "buffer_load_dword v9, s[40:43], s38 offen sc1 lds\n"
      "s_add_u32 s50, s50, 294912\n"
      "s_min_u32 s50, s50, 18579456\n"
      "s_add_u32 s53, s53, 36864\n"
      "s_add_u32 s54, s54, 21504\n"
      "s_cmp_eq_u32 s54, 129024\n"
      "s_cselect_b32 s54, 0, s54\n"
      "s_add_u32 s58, s54, s36\n"
      "s_add_u32 s59, s54, s61\n"
      "s_add_u32 m0, s58, 0\n"
      "s_nop 0\n"
      "buffer_load_dwordx4 v5, s[44:47], s50 offen nt lds\n"
      "s_add_u32 m0, s58, 4096\n"
      "s_nop 0\n"
      "buffer_load_dwordx4 v6, s[44:47], s50 offen nt lds\n"
      "s_add_u32 m0, s58, 8192\n"
      "s_nop 0\n"
      "buffer_load_dwordx4 v7, s[44:47], s50 offen nt lds\n"
      "s_add_u32 m0, s58, 12288\n"
      "s_add_u32 s38, s53, 0\n"
      "buffer_load_dwordx4 v8, s[40:43], s38 offen sc1 lds\n"
      "s_add_u32 m0, s58, 16384\n"
      "s_add_u32 s38, s53, 4096\n"
      "buffer_load_dwordx4 v8, s[40:43], s38 offen sc1 lds\n"
      "s_add_u32 m0, s59, 20480\n"
      "s_add_u32 s38, s53, 8192\n"
      "buffer_load_dword v9, s[40:43], s38 offen sc1 lds\n"
      "s_add_u32 s50, s50, 294912\n"
      "s_min_u32 s50, s50, 18579456\n"
      "s_add_u32 s53, s53, 36864\n"
      "s_add_u32 s54, s54, 21504\n"
      "s_cmp_eq_u32 s54, 129024\n"
      "s_cselect_b32 s54, 0, s54\n"
      "s_add_u32 s58, s54, s36\n"
      "s_add_u32 s59, s54, s61\n"
      "s_add_u32 m0, s58, 0\n"
      "s_nop 0\n"
      "buffer_load_dwordx4 v5, s[44:47], s50 offen nt lds\n"
      "s_add_u32 m0, s58, 4096\n"
      "s_nop 0\n"
      "buffer_load_dwordx4 v6, s[44:47], s50 offen nt lds\n"
      "s_add_u32 m0, s58, 8192\n"
      "s_nop 0\n"
      "buffer_load_dwordx4 v7, s[44:47], s50 offen nt lds\n"
      "s_add_u32 m0, s58, 12288\n"
      "s_add_u32 s38, s53, 0\n"
      "buffer_load_dwordx4 v8, s[40:43], s38 offen sc1 lds\n"
      "s_add_u32 m0, s58, 16384\n"
      "s_add_u32 s38, s53, 4096\n"
      "buffer_load_dwordx4 v8, s[40:43], s38 offen sc1 lds\n"
      "s_add_u32 m0, s59, 20480\n"
      "s_add_u32 s38, s53, 8192\n"
      "buffer_load_dword v9, s[40:43], s38 offen sc1 lds\n"
      "s_add_u32 s50, s50, 294912\n"
      "s_min_u32 s50, s50, 18579456\n"
      "s_add_u32 s53, s53, 36864\n"
      "s_add_u32 s54, s54, 21504\n"
      "s_cmp_eq_u32 s54, 129024\n"
      "s_cselect_b32 s54, 0, s54\n"
      "s_add_u32 s58, s54, s36\n"
      "s_add_u32 s59, s54, s61\n"
      "s_add_u32 m0, s58, 0\n"
      "s_nop 0\n"
      "buffer_load_dwordx4 v5, s[44:47], s50 offen nt lds\n"
      "s_add_u32 m0, s58, 4096\n"
      "s_nop 0\n"
      "buffer_load_dwordx4 v6, s[44:47], s50 offen nt lds\n"
      "s_add_u32 m0, s58, 8192\n"
      "s_nop 0\n"
      "buffer_load_dwordx4 v7, s[44:47], s50 offen nt lds\n"
      "s_add_u32 m0, s58, 12288\n"
      "s_add_u32 s38, s53, 0\n"
      "buffer_load_dwordx4 v8, s[40:43], s38 offen sc1 lds\n"
      "s_add_u32 m0, s58, 16384\n"
      "s_add_u32 s38, s53, 4096\n"
      "buffer_load_dwordx4 v8, s[40:43], s38 offen sc1 lds\n"
      "s_add_u32 m0, s59, 20480\n"
      "s_add_u32 s38, s53, 8192\n"
      "buffer_load_dword v9, s[40:43], s38 offen sc1 lds\n"
      "s_add_u32 s50, s50, 294912\n"
      "s_min_u32 s50, s50, 18579456\n"
      "s_add_u32 s53, s53, 36864\n"
      "s_add_u32 s54, s54, 21504\n"
      "s_cmp_eq_u32 s54, 129024\n"
      "s_cselect_b32 s54, 0, s54\n"
      "s_add_u32 s58, s54, s36\n"
      "s_add_u32 s59, s54, s61\n"
      "s_add_u32 m0, s58, 0\n"
      "s_nop 0\n"
      "buffer_load_dwordx4 v5, s[44:47], s50 offen nt lds\n"
      "s_add_u32 m0, s58, 4096\n"
      "s_nop 0\n"
      "buffer_load_dwordx4 v6, s[44:47], s50 offen nt lds\n"
      "s_add_u32 m0, s58, 8192\n"
      "s_nop 0\n"
      "buffer_load_dwordx4 v7, s[44:47], s50 offen nt lds\n"
      "s_add_u32 m0, s58, 12288\n"
      "s_add_u32 s38, s53, 0\n"
      "buffer_load_dwordx4 v8, s[40:43], s38 offen sc1 lds\n"
      "s_add_u32 m0, s58, 16384\n"
      "s_add_u32 s38, s53, 4096\n"
      "buffer_load_dwordx4 v8, s[40:43], s38 offen sc1 lds\n"
      "s_add_u32 m0, s59, 20480\n"
      "s_add_u32 s38, s53, 8192\n"
      "buffer_load_dword v9, s[40:43], s38 offen sc1 lds\n"
      "s_add_u32 s50, s50, 294912\n"
      "s_min_u32 s50, s50, 18579456\n"
      "s_add_u32 s53, s53, 36864\n"
      "s_add_u32 s54, s54, 21504\n"
      "s_cmp_eq_u32 s54, 129024\n"
      "s_cselect_b32 s54, 0, s54\n"
      "s_waitcnt vmcnt(30)\n"
      "s_barrier\n"
      "v_add_u32 v16, s55, v10\n"
      "v_add_u32 v17, s55, v11\n"
      "v_add_u32 v18, s55, v12\n"
      "v_add_u32 v19, s55, v13\n"
      "s_add_u32 s55, s55, 21504\n"
      "s_cmp_eq_u32 s55, 129024\n"
      "s_cselect_b32 s55, 0, s55\n"
      "ds_read_b64_tr_b16 v[24:25], v16 offset:0\n"
      "ds_read_b64_tr_b16 v[26:27], v16 offset:1536\n"
      "ds_read_b64_tr_b16 v[28:29], v17 offset:0\n"
      "ds_read_b64_tr_b16 v[30:31], v17 offset:1536\n"
      "ds_read_b64_tr_b16 v[32:33], v18 offset:0\n"
      "ds_read_b64_tr_b16 v[34:35], v18 offset:1536\n"
      "ds_read_b128 v[36:39], v19 offset:12288\n"
      "ds_read_b128 v[40:43], v19 offset:13312\n"
      "ds_read_b128 v[44:47], v19 offset:14336\n"
      "ds_read_b128 v[48:51], v19 offset:15360\n"
      "ds_read_b128 v[52:55], v19 offset:16384\n"
      "ds_read_b128 v[56:59], v19 offset:17408\n"
      "ds_read_b128 v[60:63], v19 offset:18432\n"
      "ds_read_b128 v[64:67], v19 offset:19456\n"
      "ds_read_b128 v[68:71], v19 offset:20480\n"
      "g1top69_%=:\n"
      "s_waitcnt vmcnt(24) lgkmcnt(0)\n"
      "s_barrier\n"
      "v_add_u32 v16, s55, v10\n"
      "v_add_u32 v17, s55, v11\n"
      "v_add_u32 v18, s55, v12\n"
      "v_add_u32 v19, s55, v13\n"
      "s_add_u32 s55, s55, 21504\n"
      "s_cmp_eq_u32 s55, 129024\n"
      "s_cselect_b32 s55, 0, s55\n"
      "v_mfma_f32_16x16x32_bf16 v[128:131], v[24:27], v[36:39], v[128:131]\n"
      "ds_read_b64_tr_b16 v[72:73], v16 offset:0\n"
      "v_mfma_f32_16x16x32_bf16 v[132:135], v[24:27], v[40:43], v[132:135]\n"
      "ds_read_b64_tr_b16 v[74:75], v16 offset:1536\n"
      "s_add_u32 s58, s54, s36\n"
      "s_add_u32 s59, s54, s61\n"
      "s_add_u32 m0, s58, 0\n"
      "s_nop 0\n"
      "buffer_load_dwordx4 v5, s[44:47], s50 offen nt lds\n"
      "v_mfma_f32_16x16x32_bf16 v[136:139], v[24:27], v[44:47], v[136:139]\n"
      "ds_read_b64_tr_b16 v[76:77], v17 offset:0\n"
      "v_mfma_f32_16x16x32_bf16 v[140:143], v[24:27], v[48:51], v[140:143]\n"
      "ds_read_b64_tr_b16 v[78:79], v17 offset:1536\n"
      "v_mfma_f32_16x16x32_bf16 v[144:147], v[24:27], v[52:55], v[144:147]\n"
      "ds_read_b64_tr_b16 v[80:81], v18 offset:0\n"
      "v_mfma_f32_16x16x32_bf16 v[148:151], v[24:27], v[56:59], v[148:151]\n"
      "ds_read_b64_tr_b16 v[82:83], v18 offset:1536\n"
      "s_add_u32 m0, s58, 4096\n"
      "s_nop 0\n"
      "buffer_load_dwordx4 v6, s[44:47], s50 offen nt lds\n"
      "v_mfma_f32_16x16x32_bf16 v[152:155], v[24:27], v[60:63], v[152:155]\n"
      "ds_read_b128 v[84:87], v19 offset:12288\n"
      "v_mfma_f32_16x16x32_bf16 v[156:159], v[24:27], v[64:67], v[156:159]\n"
      "ds_read_b128 v[88:91], v19 offset:13312\n"
      "v_mfma_f32_16x16x32_bf16 v[160:163], v[24:27], v[68:71], v[160:163]\n"
      "ds_read_b128 v[92:95], v19 offset:14336\n"
      "v_mfma_f32_16x16x32_bf16 v[164:167], v[28:31], v[36:39], v[164:167]\n"
      "ds_read_b128 v[96:99], v19 offset:15360\n"
      "s_add_u32 m0, s58, 8192\n"
      "s_nop 0\n"
      "buffer_load_dwordx4 v7, s[44:47], s50 offen nt lds\n"
      "v_mfma_f32_16x16x32_bf16 v[168:171], v[28:31], v[40:43], v[168:171]\n"
      "ds_read_b128 v[100:103], v19 offset:16384\n"
      "v_mfma_f32_16x16x32_bf16 v[172:175], v[28:31], v[44:47], v[172:175]\n"
      "ds_read_b128 v[104:107], v19 offset:17408\n"
      "v_mfma_f32_16x16x32_bf16 v[176:179], v[28:31], v[48:51], v[176:179]\n"
      "ds_read_b128 v[108:111], v19 offset:18432\n"
      "v_mfma_f32_16x16x32_bf16 v[180:183], v[28:31], v[52:55], v[180:183]\n"
      "ds_read_b128 v[112:115], v19 offset:19456\n"
      "s_add_u32 m0, s58, 12288\n"
      "s_add_u32 s38, s53, 0\n"
      "buffer_load_dwordx4 v8, s[40:43], s38 offen sc1 lds\n"
      "v_mfma_f32_16x16x32_bf16 v[184:187], v[28:31], v[56:59], v[184:187]\n"
      "ds_read_b128 v[116:119], v19 offset:20480\n"
      "v_mfma_f32_16x16x32_bf16 v[188:191], v[28:31], v[60:63], v[188:191]\n"
      "v_mfma_f32_16x16x32_bf16 v[192:195], v[28:31], v[64:67], v[192:195]\n"
      "v_mfma_f32_16x16x32_bf16 v[196:199], v[28:31], v[68:71], v[196:199]\n"
      "s_add_u32 m0, s58, 16384\n"
      "s_add_u32 s38, s53, 4096\n"
      "buffer_load_dwordx4 v8, s[40:43], s38 offen sc1 lds\n"
      "v_mfma_f32_16x16x32_bf16 v[200:203], v[32:35], v[36:39], v[200:203]\n"
      "v_mfma_f32_16x16x32_bf16 v[204:207], v[32:35], v[40:43], v[204:207]\n"
      "v_mfma_f32_16x16x32_bf16 v[208:211], v[32:35], v[44:47], v[208:211]\n"
      "v_mfma_f32_16x16x32_bf16 v[212:215], v[32:35], v[48:51], v[212:215]\n"
      "s_add_u32 m0, s59, 20480\n"
      "s_add_u32 s38, s53, 8192\n"
      "buffer_load_dword v9, s[40:43], s38 offen sc1 lds\n"
      "s_add_u32 s50, s50, 294912\n"
      "s_min_u32 s50, s50, 18579456\n"
      "s_add_u32 s53, s53, 36864\n"
      "s_add_u32 s54, s54, 21504\n"
      "s_cmp_eq_u32 s54, 129024\n"
      "s_cselect_b32 s54, 0, s54\n"
      "v_mfma_f32_16x16x32_bf16 v[216:219], v[32:35], v[52:55], v[216:219]\n"
      "v_mfma_f32_16x16x32_bf16 v[220:223], v[32:35], v[56:59], v[220:223]\n"
      "v_mfma_f32_16x16x32_bf16 v[224:227], v[32:35], v[60:63], v[224:227]\n"
      "v_mfma_f32_16x16x32_bf16 v[228:231], v[32:35], v[64:67], v[228:231]\n"
      "v_mfma_f32_16x16x32_bf16 v[232:235], v[32:35], v[68:71], v[232:235]\n"
      "s_waitcnt vmcnt(24) lgkmcnt(0)\n"
      "s_barrier\n"
      "v_add_u32 v16, s55, v10\n"
      "v_add_u32 v17, s55, v11\n"
      "v_add_u32 v18, s55, v12\n"
      "v_add_u32 v19, s55, v13\n"
      "s_add_u32 s55, s55, 21504\n"
      "s_cmp_eq_u32 s55, 129024\n"
      "s_cselect_b32 s55, 0, s55\n"
      "v_mfma_f32_16x16x32_bf16 v[128:131], v[72:75], v[84:87], v[128:131]\n"
      "ds_read_b64_tr_b16 v[24:25], v16 offset:0\n"
      "v_mfma_f32_16x16x32_bf16 v[132:135], v[72:75], v[88:91], v[132:135]\n"
      "ds_read_b64_tr_b16 v[26:27], v16 offset:1536\n"
      "s_add_u32 s58, s54, s36\n"
      "s_add_u32 s59, s54, s61\n"
      "s_add_u32 m0, s58, 0\n"
      "s_nop 0\n"
      "buffer_load_dwordx4 v5, s[44:47], s50 offen nt lds\n"
      "v_mfma_f32_16x16x32_bf16 v[136:139], v[72:75], v[92:95], v[136:139]\n"
      "ds_read_b64_tr_b16 v[28:29], v17 offset:0\n"
      "v_mfma_f32_16x16x32_bf16 v[140:143], v[72:75], v[96:99], v[140:143]\n"
      "ds_read_b64_tr_b16 v[30:31], v17 offset:1536\n"
      "v_mfma_f32_16x16x32_bf16 v[144:147], v[72:75], v[100:103], v[144:147]\n"
      "ds_read_b64_tr_b16 v[32:33], v18 offset:0\n"
      "v_mfma_f32_16x16x32_bf16 v[148:151], v[72:75], v[104:107], v[148:151]\n"
      "ds_read_b64_tr_b16 v[34:35], v18 offset:1536\n"
      "s_add_u32 m0, s58, 4096\n"
      "s_nop 0\n"
      "buffer_load_dwordx4 v6, s[44:47], s50 offen nt lds\n"
      "v_mfma_f32_16x16x32_bf16 v[152:155], v[72:75], v[108:111], v[152:155]\n"
      "ds_read_b128 v[36:39], v19 offset:12288\n"
      "v_mfma_f32_16x16x32_bf16 v[156:159], v[72:75], v[112:115], v[156:159]\n"
      "ds_read_b128 v[40:43], v19 offset:13312\n"
      "v_mfma_f32_16x16x32_bf16 v[160:163], v[72:75], v[116:119], v[160:163]\n"
      "ds_read_b128 v[44:47], v19 offset:14336\n"
      "v_mfma_f32_16x16x32_bf16 v[164:167], v[76:79], v[84:87], v[164:167]\n"
      "ds_read_b128 v[48:51], v19 offset:15360\n"
      "s_add_u32 m0, s58, 8192\n"
      "s_nop 0\n"
      "buffer_load_dwordx4 v7, s[44:47], s50 offen nt lds\n"
      "v_mfma_f32_16x16x32_bf16 v[168:171], v[76:79], v[88:91], v[168:171]\n"
      "ds_read_b128 v[52:55], v19 offset:16384\n"
      "v_mfma_f32_16x16x32_bf16 v[172:175], v[76:79], v[92:95], v[172:175]\n"
      "ds_read_b128 v[56:59], v19 offset:17408\n"
      "v_mfma_f32_16x16x32_bf16 v[176:179], v[76:79], v[96:99], v[176:179]\n"
      "ds_read_b128 v[60:63], v19 offset:18432\n"
      "v_mfma_f32_16x16x32_bf16 v[180:183], v[76:79], v[100:103], v[180:183]\n"
      "ds_read_b128 v[64:67], v19 offset:19456\n"
      "s_add_u32 m0, s58, 12288\n"
      "s_add_u32 s38, s53, 0\n"
      "buffer_load_dwordx4 v8, s[40:43], s38 offen sc1 lds\n"
      "v_mfma_f32_16x16x32_bf16 v[184:187], v[76:79], v[104:107], v[184:187]\n"
      "ds_read_b128 v[68:71], v19 offset:20480\n"
      "v_mfma_f32_16x16x32_bf16 v[188:191], v[76:79], v[108:111], v[188:191]\n"
      "v_mfma_f32_16x16x32_bf16 v[192:195], v[76:79], v[112:115], v[192:195]\n"
      "v_mfma_f32_16x16x32_bf16 v[196:199], v[76:79], v[116:119], v[196:199]\n"
      "s_add_u32 m0, s58, 16384\n"
      "s_add_u32 s38, s53, 4096\n"
      "buffer_load_dwordx4 v8, s[40:43], s38 offen sc1 lds\n"
      "v_mfma_f32_16x16x32_bf16 v[200:203], v[80:83], v[84:87], v[200:203]\n"
      "v_mfma_f32_16x16x32_bf16 v[204:207], v[80:83], v[88:91], v[204:207]\n"
      "v_mfma_f32_16x16x32_bf16 v[208:211], v[80:83], v[92:95], v[208:211]\n"
      "v_mfma_f32_16x16x32_bf16 v[212:215], v[80:83], v[96:99], v[212:215]\n"
      "s_add_u32 m0, s59, 20480\n"
      "s_add_u32 s38, s53, 8192\n"
      "buffer_load_dword v9, s[40:43], s38 offen sc1 lds\n"
      "s_add_u32 s50, s50, 294912\n"
      "s_min_u32 s50, s50, 18579456\n"
      "s_add_u32 s53, s53, 36864\n"
      "s_add_u32 s54, s54, 21504\n"
      "s_cmp_eq_u32 s54, 129024\n"
      "s_cselect_b32 s54, 0, s54\n"
      "v_mfma_f32_16x16x32_bf16 v[216:219], v[80:83], v[100:103], v[216:219]\n"
      "v_mfma_f32_16x16x32_bf16 v[220:223], v[80:83], v[104:107], v[220:223]\n"
      "v_mfma_f32_16x16x32_bf16 v[224:227], v[80:83], v[108:111], v[224:227]\n"
      "v_mfma_f32_16x16x32_bf16 v[228:231], v[80:83], v[112:115], v[228:231]\n"
      "v_mfma_f32_16x16x32_bf16 v[232:235], v[80:83], v[116:119], v[232:235]\n"
      "s_add_u32 s37, s37, 2\n"
      "s_cmp_lt_u32 s37, 64\n"
      "s_cbranch_scc1 g1top69_%=\n"
      "s_waitcnt vmcnt(0) lgkmcnt(0)\n"
      "s_mov_b32 s44, %[b1lo]\n"
      "s_mov_b32 s45, %[b1hi]\n"
      "s_mov_b32 s46, 9216\n"
      "buffer_load_dwordx2 v[36:37], v14, s[44:47], 0 offen offset:0\n"
      "buffer_load_dwordx2 v[38:39], v14, s[44:47], 0 offen offset:32\n"
      "buffer_load_dwordx2 v[40:41], v14, s[44:47], 0 offen offset:64\n"
      "s_mov_b32 s62, 0xc0135761\n"
      "v_mov_b32 v20, 0xbdd2d3e8\n"
      "s_waitcnt vmcnt(0)\n"
      "v_lshlrev_b32 v24, 16, v36\n"
      "v_and_b32 v25, 0xffff0000, v36\n"
      "v_lshlrev_b32 v26, 16, v37\n"
      "v_and_b32 v27, 0xffff0000, v37\n"
      "v_lshlrev_b32 v28, 16, v38\n"
      "v_and_b32 v29, 0xffff0000, v38\n"
      "v_lshlrev_b32 v30, 16, v39\n"
      "v_and_b32 v31, 0xffff0000, v39\n"
      "v_lshlrev_b32 v32, 16, v40\n"
      "v_and_b32 v33, 0xffff0000, v40\n"
      "v_lshlrev_b32 v34, 16, v41\n"
      "v_and_b32 v35, 0xffff0000, v41\n"
      "s_mov_b32 s63, 0\n"
      "v_add_f32 v128, v128, v24\n"
      "v_add_f32 v129, v129, v25\n"
      "v_add_f32 v130, v130, v26\n"
      "v_add_f32 v131, v131, v27\n"
      "v_mul_f32 v44, v128, v128\n"
      "v_mul_f32 v45, v129, v129\n"
      "v_mul_f32 v46, v130, v130\n"
      "v_mul_f32 v47, v131, v131\n"
      "v_fma_f32 v44, v44, v20, s62\n"
      "v_fma_f32 v45, v45, v20, s62\n"
      "v_fma_f32 v46, v46, v20, s62\n"
      "v_fma_f32 v47, v47, v20, s62\n"
      "v_mul_f32 v44, v44, v128\n"
      "v_mul_f32 v45, v45, v129\n"
      "v_mul_f32 v46, v46, v130\n"
      "v_mul_f32 v47, v47, v131\n"
      "v_exp_f32 v44, v44\n"
      "v_exp_f32 v45, v45\n"
      "v_exp_f32 v46, v46\n"
      "v_exp_f32 v47, v47\n"
      "v_add_f32 v44, 1.0, v44\n"
      "v_add_f32 v45, 1.0, v45\n"
      "v_add_f32 v46, 1.0, v46\n"
      "v_add_f32 v47, 1.0, v47\n"
      "v_rcp_f32 v44, v44\n"
      "v_rcp_f32 v45, v45\n"
      "v_rcp_f32 v46, v46\n"
      "v_rcp_f32 v47, v47\n"
      "v_mul_f32 v44, v44, v128\n"
      "v_mul_f32 v45, v45, v129\n"
      "v_mul_f32 v46, v46, v130\n"
      "v_mul_f32 v47, v47, v131\n"
      "v_cvt_pk_bf16_f32 v52, v44, v45\n"
      "v_cvt_pk_bf16_f32 v53, v46, v47\n"
      "buffer_store_dwordx2 v[52:53], v15, s[40:43], s63 offen sc1\n"
      "s_add_u32 s63, s63, 1024\n"
      "v_add_f32 v132, v132, v24\n"
      "v_add_f32 v133, v133, v25\n"
      "v_add_f32 v134, v134, v26\n"
      "v_add_f32 v135, v135, v27\n"
      "v_mul_f32 v48, v132, v132\n"
      "v_mul_f32 v49, v133, v133\n"
      "v_mul_f32 v50, v134, v134\n"
      "v_mul_f32 v51, v135, v135\n"
      "v_fma_f32 v48, v48, v20, s62\n"
      "v_fma_f32 v49, v49, v20, s62\n"
      "v_fma_f32 v50, v50, v20, s62\n"
      "v_fma_f32 v51, v51, v20, s62\n"
      "v_mul_f32 v48, v48, v132\n"
      "v_mul_f32 v49, v49, v133\n"
      "v_mul_f32 v50, v50, v134\n"
      "v_mul_f32 v51, v51, v135\n"
      "v_exp_f32 v48, v48\n"
      "v_exp_f32 v49, v49\n"
      "v_exp_f32 v50, v50\n"
      "v_exp_f32 v51, v51\n"
      "v_add_f32 v48, 1.0, v48\n"
      "v_add_f32 v49, 1.0, v49\n"
      "v_add_f32 v50, 1.0, v50\n"
      "v_add_f32 v51, 1.0, v51\n"
      "v_rcp_f32 v48, v48\n"
      "v_rcp_f32 v49, v49\n"
      "v_rcp_f32 v50, v50\n"
      "v_rcp_f32 v51, v51\n"
      "v_mul_f32 v48, v48, v132\n"
      "v_mul_f32 v49, v49, v133\n"
      "v_mul_f32 v50, v50, v134\n"
      "v_mul_f32 v51, v51, v135\n"
      "v_cvt_pk_bf16_f32 v54, v48, v49\n"
      "v_cvt_pk_bf16_f32 v55, v50, v51\n"
      "buffer_store_dwordx2 v[54:55], v15, s[40:43], s63 offen sc1\n"
      "s_add_u32 s63, s63, 1024\n"
      "v_add_f32 v136, v136, v24\n"
      "v_add_f32 v137, v137, v25\n"
      "v_add_f32 v138, v138, v26\n"
      "v_add_f32 v139, v139, v27\n"
      "v_mul_f32 v44, v136, v136\n"
      "v_mul_f32 v45, v137, v137\n"
      "v_mul_f32 v46, v138, v138\n"
      "v_mul_f32 v47, v139, v139\n"
      "v_fma_f32 v44, v44, v20, s62\n"
      "v_fma_f32 v45, v45, v20, s62\n"
      "v_fma_f32 v46, v46, v20, s62\n"
      "v_fma_f32 v47, v47, v20, s62\n"
      "v_mul_f32 v44, v44, v136\n"
      "v_mul_f32 v45, v45, v137\n"
      "v_mul_f32 v46, v46, v138\n"
      "v_mul_f32 v47, v47, v139\n"
      "v_exp_f32 v44, v44\n"
      "v_exp_f32 v45, v45\n"
      "v_exp_f32 v46, v46\n"
      "v_exp_f32 v47, v47\n"
      "v_add_f32 v44, 1.0, v44\n"
      "v_add_f32 v45, 1.0, v45\n"
      "v_add_f32 v46, 1.0, v46\n"
      "v_add_f32 v47, 1.0, v47\n"
      "v_rcp_f32 v44, v44\n"
      "v_rcp_f32 v45, v45\n"
      "v_rcp_f32 v46, v46\n"
      "v_rcp_f32 v47, v47\n"
      "v_mul_f32 v44, v44, v136\n"
      "v_mul_f32 v45, v45, v137\n"
      "v_mul_f32 v46, v46, v138\n"
      "v_mul_f32 v47, v47, v139\n"
      "v_cvt_pk_bf16_f32 v56, v44, v45\n"
      "v_cvt_pk_bf16_f32 v57, v46, v47\n"
      "buffer_store_dwordx2 v[56:57], v15, s[40:43], s63 offen sc1\n"
      "s_add_u32 s63, s63, 1024\n"
      "v_add_f32 v140, v140, v24\n"
      "v_add_f32 v141, v141, v25\n"
      "v_add_f32 v142, v142, v26\n"
      "v_add_f32 v143, v143, v27\n"
      "v_mul_f32 v48, v140, v140\n"
      "v_mul_f32 v49, v141, v141\n"
      "v_mul_f32 v50, v142, v142\n"
      "v_mul_f32 v51, v143, v143\n"
      "v_fma_f32 v48, v48, v20, s62\n"
      "v_fma_f32 v49, v49, v20, s62\n"
      "v_fma_f32 v50, v50, v20, s62\n"
      "v_fma_f32 v51, v51, v20, s62\n"
      "v_mul_f32 v48, v48, v140\n"
      "v_mul_f32 v49, v49, v141\n"
      "v_mul_f32 v50, v50, v142\n"
      "v_mul_f32 v51, v51, v143\n"
      "v_exp_f32 v48, v48\n"
      "v_exp_f32 v49, v49\n"
      "v_exp_f32 v50, v50\n"
      "v_exp_f32 v51, v51\n"
      "v_add_f32 v48, 1.0, v48\n"
      "v_add_f32 v49, 1.0, v49\n"
      "v_add_f32 v50, 1.0, v50\n"
      "v_add_f32 v51, 1.0, v51\n"
      "v_rcp_f32 v48, v48\n"
      "v_rcp_f32 v49, v49\n"
      "v_rcp_f32 v50, v50\n"
      "v_rcp_f32 v51, v51\n"
      "v_mul_f32 v48, v48, v140\n"
      "v_mul_f32 v49, v49, v141\n"
      "v_mul_f32 v50, v50, v142\n"
      "v_mul_f32 v51, v51, v143\n"
      "v_cvt_pk_bf16_f32 v58, v48, v49\n"
      "v_cvt_pk_bf16_f32 v59, v50, v51\n"
      "buffer_store_dwordx2 v[58:59], v15, s[40:43], s63 offen sc1\n"
      "s_add_u32 s63, s63, 1024\n"
      "v_add_f32 v144, v144, v24\n"
      "v_add_f32 v145, v145, v25\n"
      "v_add_f32 v146, v146, v26\n"
      "v_add_f32 v147, v147, v27\n"
      "v_mul_f32 v44, v144, v144\n"
      "v_mul_f32 v45, v145, v145\n"
      "v_mul_f32 v46, v146, v146\n"
      "v_mul_f32 v47, v147, v147\n"
      "v_fma_f32 v44, v44, v20, s62\n"
      "v_fma_f32 v45, v45, v20, s62\n"
      "v_fma_f32 v46, v46, v20, s62\n"
      "v_fma_f32 v47, v47, v20, s62\n"
      "v_mul_f32 v44, v44, v144\n"
      "v_mul_f32 v45, v45, v145\n"
      "v_mul_f32 v46, v46, v146\n"
      "v_mul_f32 v47, v47, v147\n"
      "v_exp_f32 v44, v44\n"
      "v_exp_f32 v45, v45\n"
      "v_exp_f32 v46, v46\n"
      "v_exp_f32 v47, v47\n"
      "v_add_f32 v44, 1.0, v44\n"
      "v_add_f32 v45, 1.0, v45\n"
      "v_add_f32 v46, 1.0, v46\n"
      "v_add_f32 v47, 1.0, v47\n"
      "v_rcp_f32 v44, v44\n"
      "v_rcp_f32 v45, v45\n"
      "v_rcp_f32 v46, v46\n"
      "v_rcp_f32 v47, v47\n"
      "v_mul_f32 v44, v44, v144\n"
      "v_mul_f32 v45, v45, v145\n"
      "v_mul_f32 v46, v46, v146\n"
      "v_mul_f32 v47, v47, v147\n"
      "v_cvt_pk_bf16_f32 v52, v44, v45\n"
      "v_cvt_pk_bf16_f32 v53, v46, v47\n"
      "buffer_store_dwordx2 v[52:53], v15, s[40:43], s63 offen sc1\n"
      "s_add_u32 s63, s63, 1024\n"
      "v_add_f32 v148, v148, v24\n"
      "v_add_f32 v149, v149, v25\n"
      "v_add_f32 v150, v150, v26\n"
      "v_add_f32 v151, v151, v27\n"
      "v_mul_f32 v48, v148, v148\n"
      "v_mul_f32 v49, v149, v149\n"
      "v_mul_f32 v50, v150, v150\n"
      "v_mul_f32 v51, v151, v151\n"
      "v_fma_f32 v48, v48, v20, s62\n"
      "v_fma_f32 v49, v49, v20, s62\n"
      "v_fma_f32 v50, v50, v20, s62\n"
      "v_fma_f32 v51, v51, v20, s62\n"
      "v_mul_f32 v48, v48, v148\n"
      "v_mul_f32 v49, v49, v149\n"
      "v_mul_f32 v50, v50, v150\n"
      "v_mul_f32 v51, v51, v151\n"
      "v_exp_f32 v48, v48\n"
      "v_exp_f32 v49, v49\n"
      "v_exp_f32 v50, v50\n"
      "v_exp_f32 v51, v51\n"
      "v_add_f32 v48, 1.0, v48\n"
      "v_add_f32 v49, 1.0, v49\n"
      "v_add_f32 v50, 1.0, v50\n"
      "v_add_f32 v51, 1.0, v51\n"
      "v_rcp_f32 v48, v48\n"
      "v_rcp_f32 v49, v49\n"
      "v_rcp_f32 v50, v50\n"
      "v_rcp_f32 v51, v51\n"
      "v_mul_f32 v48, v48, v148\n"
      "v_mul_f32 v49, v49, v149\n"
      "v_mul_f32 v50, v50, v150\n"
      "v_mul_f32 v51, v51, v151\n"
      "v_cvt_pk_bf16_f32 v54, v48, v49\n"
      "v_cvt_pk_bf16_f32 v55, v50, v51\n"
      "buffer_store_dwordx2 v[54:55], v15, s[40:43], s63 offen sc1\n"
      "s_add_u32 s63, s63, 1024\n"
      "v_add_f32 v152, v152, v24\n"
      "v_add_f32 v153, v153, v25\n"
      "v_add_f32 v154, v154, v26\n"
      "v_add_f32 v155, v155, v27\n"
      "v_mul_f32 v44, v152, v152\n"
      "v_mul_f32 v45, v153, v153\n"
      "v_mul_f32 v46, v154, v154\n"
      "v_mul_f32 v47, v155, v155\n"
      "v_fma_f32 v44, v44, v20, s62\n"
      "v_fma_f32 v45, v45, v20, s62\n"
      "v_fma_f32 v46, v46, v20, s62\n"
      "v_fma_f32 v47, v47, v20, s62\n"
      "v_mul_f32 v44, v44, v152\n"
      "v_mul_f32 v45, v45, v153\n"
      "v_mul_f32 v46, v46, v154\n"
      "v_mul_f32 v47, v47, v155\n"
      "v_exp_f32 v44, v44\n"
      "v_exp_f32 v45, v45\n"
      "v_exp_f32 v46, v46\n"
      "v_exp_f32 v47, v47\n"
      "v_add_f32 v44, 1.0, v44\n"
      "v_add_f32 v45, 1.0, v45\n"
      "v_add_f32 v46, 1.0, v46\n"
      "v_add_f32 v47, 1.0, v47\n"
      "v_rcp_f32 v44, v44\n"
      "v_rcp_f32 v45, v45\n"
      "v_rcp_f32 v46, v46\n"
      "v_rcp_f32 v47, v47\n"
      "v_mul_f32 v44, v44, v152\n"
      "v_mul_f32 v45, v45, v153\n"
      "v_mul_f32 v46, v46, v154\n"
      "v_mul_f32 v47, v47, v155\n"
      "v_cvt_pk_bf16_f32 v56, v44, v45\n"
      "v_cvt_pk_bf16_f32 v57, v46, v47\n"
      "buffer_store_dwordx2 v[56:57], v15, s[40:43], s63 offen sc1\n"
      "s_add_u32 s63, s63, 1024\n"
      "v_add_f32 v156, v156, v24\n"
      "v_add_f32 v157, v157, v25\n"
      "v_add_f32 v158, v158, v26\n"
      "v_add_f32 v159, v159, v27\n"
      "v_mul_f32 v48, v156, v156\n"
      "v_mul_f32 v49, v157, v157\n"
      "v_mul_f32 v50, v158, v158\n"
      "v_mul_f32 v51, v159, v159\n"
      "v_fma_f32 v48, v48, v20, s62\n"
      "v_fma_f32 v49, v49, v20, s62\n"
      "v_fma_f32 v50, v50, v20, s62\n"
      "v_fma_f32 v51, v51, v20, s62\n"
      "v_mul_f32 v48, v48, v156\n"
      "v_mul_f32 v49, v49, v157\n"
      "v_mul_f32 v50, v50, v158\n"
      "v_mul_f32 v51, v51, v159\n"
      "v_exp_f32 v48, v48\n"
      "v_exp_f32 v49, v49\n"
      "v_exp_f32 v50, v50\n"
      "v_exp_f32 v51, v51\n"
      "v_add_f32 v48, 1.0, v48\n"
      "v_add_f32 v49, 1.0, v49\n"
      "v_add_f32 v50, 1.0, v50\n"
      "v_add_f32 v51, 1.0, v51\n"
      "v_rcp_f32 v48, v48\n"
      "v_rcp_f32 v49, v49\n"
      "v_rcp_f32 v50, v50\n"
      "v_rcp_f32 v51, v51\n"
      "v_mul_f32 v48, v48, v156\n"
      "v_mul_f32 v49, v49, v157\n"
      "v_mul_f32 v50, v50, v158\n"
      "v_mul_f32 v51, v51, v159\n"
      "v_cvt_pk_bf16_f32 v58, v48, v49\n"
      "v_cvt_pk_bf16_f32 v59, v50, v51\n"
      "buffer_store_dwordx2 v[58:59], v15, s[40:43], s63 offen sc1\n"
      "s_add_u32 s63, s63, 1024\n"
      "v_add_f32 v160, v160, v24\n"
      "v_add_f32 v161, v161, v25\n"
      "v_add_f32 v162, v162, v26\n"
      "v_add_f32 v163, v163, v27\n"
      "v_mul_f32 v44, v160, v160\n"
      "v_mul_f32 v45, v161, v161\n"
      "v_mul_f32 v46, v162, v162\n"
      "v_mul_f32 v47, v163, v163\n"
      "v_fma_f32 v44, v44, v20, s62\n"
      "v_fma_f32 v45, v45, v20, s62\n"
      "v_fma_f32 v46, v46, v20, s62\n"
      "v_fma_f32 v47, v47, v20, s62\n"
      "v_mul_f32 v44, v44, v160\n"
      "v_mul_f32 v45, v45, v161\n"
      "v_mul_f32 v46, v46, v162\n"
      "v_mul_f32 v47, v47, v163\n"
      "v_exp_f32 v44, v44\n"
      "v_exp_f32 v45, v45\n"
      "v_exp_f32 v46, v46\n"
      "v_exp_f32 v47, v47\n"
      "v_add_f32 v44, 1.0, v44\n"
      "v_add_f32 v45, 1.0, v45\n"
      "v_add_f32 v46, 1.0, v46\n"
      "v_add_f32 v47, 1.0, v47\n"
      "v_rcp_f32 v44, v44\n"
      "v_rcp_f32 v45, v45\n"
      "v_rcp_f32 v46, v46\n"
      "v_rcp_f32 v47, v47\n"
      "v_mul_f32 v44, v44, v160\n"
      "v_mul_f32 v45, v45, v161\n"
      "v_mul_f32 v46, v46, v162\n"
      "v_mul_f32 v47, v47, v163\n"
      "v_cvt_pk_bf16_f32 v52, v44, v45\n"
      "v_cvt_pk_bf16_f32 v53, v46, v47\n"
      "buffer_store_dwordx2 v[52:53], v15, s[40:43], s63 offen sc1\n"
      "s_add_u32 s63, s63, 1024\n"
      "s_bitcmp1_b32 s36, 10\n"
      "s_cselect_b32 s63, 36864-1024, 0\n"
      "s_add_u32 s63, s63, 512\n"
      "v_add_f32 v164, v164, v28\n"
      "v_add_f32 v165, v165, v29\n"
      "v_add_f32 v166, v166, v30\n"
      "v_add_f32 v167, v167, v31\n"
      "v_mul_f32 v48, v164, v164\n"
      "v_mul_f32 v49, v165, v165\n"
      "v_mul_f32 v50, v166, v166\n"
      "v_mul_f32 v51, v167, v167\n"
      "v_fma_f32 v48, v48, v20, s62\n"
      "v_fma_f32 v49, v49, v20, s62\n"
      "v_fma_f32 v50, v50, v20, s62\n"
      "v_fma_f32 v51, v51, v20, s62\n"
      "v_mul_f32 v48, v48, v164\n"
      "v_mul_f32 v49, v49, v165\n"
      "v_mul_f32 v50, v50, v166\n"
      "v_mul_f32 v51, v51, v167\n"
      "v_exp_f32 v48, v48\n"
      "v_exp_f32 v49, v49\n"
      "v_exp_f32 v50, v50\n"
      "v_exp_f32 v51, v51\n"
      "v_add_f32 v48, 1.0, v48\n"
      "v_add_f32 v49, 1.0, v49\n"
      "v_add_f32 v50, 1.0, v50\n"
      "v_add_f32 v51, 1.0, v51\n"
      "v_rcp_f32 v48, v48\n"
      "v_rcp_f32 v49, v49\n"
      "v_rcp_f32 v50, v50\n"
      "v_rcp_f32 v51, v51\n"
      "v_mul_f32 v48, v48, v164\n"
      "v_mul_f32 v49, v49, v165\n"
      "v_mul_f32 v50, v50, v166\n"
      "v_mul_f32 v51, v51, v167\n"
      "v_cvt_pk_bf16_f32 v54, v48, v49\n"
      "v_cvt_pk_bf16_f32 v55, v50, v51\n"
      "buffer_store_dwordx2 v[54:55], v15, s[40:43], s63 offen sc1\n"
      "s_add_u32 s63, s63, 1024\n"
      "v_add_f32 v168, v168, v28\n"
      "v_add_f32 v169, v169, v29\n"
      "v_add_f32 v170, v170, v30\n"
      "v_add_f32 v171, v171, v31\n"
      "v_mul_f32 v44, v168, v168\n"
      "v_mul_f32 v45, v169, v169\n"
      "v_mul_f32 v46, v170, v170\n"
      "v_mul_f32 v47, v171, v171\n"
      "v_fma_f32 v44, v44, v20, s62\n"
      "v_fma_f32 v45, v45, v20, s62\n"
      "v_fma_f32 v46, v46, v20, s62\n"
      "v_fma_f32 v47, v47, v20, s62\n"
      "v_mul_f32 v44, v44, v168\n"
      "v_mul_f32 v45, v45, v169\n"
      "v_mul_f32 v46, v46, v170\n"
      "v_mul_f32 v47, v47, v171\n"
      "v_exp_f32 v44, v44\n"
      "v_exp_f32 v45, v45\n"
      "v_exp_f32 v46, v46\n"
      "v_exp_f32 v47, v47\n"
      "v_add_f32 v44, 1.0, v44\n"
      "v_add_f32 v45, 1.0, v45\n"
      "v_add_f32 v46, 1.0, v46\n"
      "v_add_f32 v47, 1.0, v47\n"
      "v_rcp_f32 v44, v44\n"
      "v_rcp_f32 v45, v45\n"
      "v_rcp_f32 v46, v46\n"
      "v_rcp_f32 v47, v47\n"
      "v_mul_f32 v44, v44, v168\n"
      "v_mul_f32 v45, v45, v169\n"
      "v_mul_f32 v46, v46, v170\n"
      "v_mul_f32 v47, v47, v171\n"
      "v_cvt_pk_bf16_f32 v56, v44, v45\n"
      "v_cvt_pk_bf16_f32 v57, v46, v47\n"
      "buffer_store_dwordx2 v[56:57], v15, s[40:43], s63 offen sc1\n"
      "s_add_u32 s63, s63, 1024\n"
      "v_add_f32 v172, v172, v28\n"
      "v_add_f32 v173, v173, v29\n"
      "v_add_f32 v174, v174, v30\n"
      "v_add_f32 v175, v175, v31\n"
      "v_mul_f32 v48, v172, v172\n"
      "v_mul_f32 v49, v173, v173\n"
      "v_mul_f32 v50, v174, v174\n"
      "v_mul_f32 v51, v175, v175\n"
      "v_fma_f32 v48, v48, v20, s62\n"
      "v_fma_f32 v49, v49, v20, s62\n"
      "v_fma_f32 v50, v50, v20, s62\n"
      "v_fma_f32 v51, v51, v20, s62\n"
      "v_mul_f32 v48, v48, v172\n"
      "v_mul_f32 v49, v49, v173\n"
      "v_mul_f32 v50, v50, v174\n"
      "v_mul_f32 v51, v51, v175\n"
      "v_exp_f32 v48, v48\n"
      "v_exp_f32 v49, v49\n"
      "v_exp_f32 v50, v50\n"
      "v_exp_f32 v51, v51\n"
      "v_add_f32 v48, 1.0, v48\n"
      "v_add_f32 v49, 1.0, v49\n"
      "v_add_f32 v50, 1.0, v50\n"
      "v_add_f32 v51, 1.0, v51\n"
      "v_rcp_f32 v48, v48\n"
      "v_rcp_f32 v49, v49\n"
      "v_rcp_f32 v50, v50\n"
      "v_rcp_f32 v51, v51\n"
      "v_mul_f32 v48, v48, v172\n"
      "v_mul_f32 v49, v49, v173\n"
      "v_mul_f32 v50, v50, v174\n"
      "v_mul_f32 v51, v51, v175\n"
      "v_cvt_pk_bf16_f32 v58, v48, v49\n"
      "v_cvt_pk_bf16_f32 v59, v50, v51\n"
      "buffer_store_dwordx2 v[58:59], v15, s[40:43], s63 offen sc1\n"
      "s_add_u32 s63, s63, 1024\n"
      "v_add_f32 v176, v176, v28\n"
      "v_add_f32 v177, v177, v29\n"
      "v_add_f32 v178, v178, v30\n"
      "v_add_f32 v179, v179, v31\n"
      "v_mul_f32 v44, v176, v176\n"
      "v_mul_f32 v45, v177, v177\n"
      "v_mul_f32 v46, v178, v178\n"
      "v_mul_f32 v47, v179, v179\n"
      "v_fma_f32 v44, v44, v20, s62\n"
      "v_fma_f32 v45, v45, v20, s62\n"
      "v_fma_f32 v46, v46, v20, s62\n"
      "v_fma_f32 v47, v47, v20, s62\n"
      "v_mul_f32 v44, v44, v176\n"
      "v_mul_f32 v45, v45, v177\n"
      "v_mul_f32 v46, v46, v178\n"
      "v_mul_f32 v47, v47, v179\n"
      "v_exp_f32 v44, v44\n"
      "v_exp_f32 v45, v45\n"
      "v_exp_f32 v46, v46\n"
      "v_exp_f32 v47, v47\n"
      "v_add_f32 v44, 1.0, v44\n"
      "v_add_f32 v45, 1.0, v45\n"
      "v_add_f32 v46, 1.0, v46\n"
      "v_add_f32 v47, 1.0, v47\n"
      "v_rcp_f32 v44, v44\n"
      "v_rcp_f32 v45, v45\n"
      "v_rcp_f32 v46, v46\n"
      "v_rcp_f32 v47, v47\n"
      "v_mul_f32 v44, v44, v176\n"
      "v_mul_f32 v45, v45, v177\n"
      "v_mul_f32 v46, v46, v178\n"
      "v_mul_f32 v47, v47, v179\n"
      "v_cvt_pk_bf16_f32 v52, v44, v45\n"
      "v_cvt_pk_bf16_f32 v53, v46, v47\n"
      "buffer_store_dwordx2 v[52:53], v15, s[40:43], s63 offen sc1\n"
      "s_add_u32 s63, s63, 1024\n"
      "v_add_f32 v180, v180, v28\n"
      "v_add_f32 v181, v181, v29\n"
      "v_add_f32 v182, v182, v30\n"
      "v_add_f32 v183, v183, v31\n"
      "v_mul_f32 v48, v180, v180\n"
      "v_mul_f32 v49, v181, v181\n"
      "v_mul_f32 v50, v182, v182\n"
      "v_mul_f32 v51, v183, v183\n"
      "v_fma_f32 v48, v48, v20, s62\n"
      "v_fma_f32 v49, v49, v20, s62\n"
      "v_fma_f32 v50, v50, v20, s62\n"
      "v_fma_f32 v51, v51, v20, s62\n"
      "v_mul_f32 v48, v48, v180\n"
      "v_mul_f32 v49, v49, v181\n"
      "v_mul_f32 v50, v50, v182\n"
      "v_mul_f32 v51, v51, v183\n"
      "v_exp_f32 v48, v48\n"
      "v_exp_f32 v49, v49\n"
      "v_exp_f32 v50, v50\n"
      "v_exp_f32 v51, v51\n"
      "v_add_f32 v48, 1.0, v48\n"
      "v_add_f32 v49, 1.0, v49\n"
      "v_add_f32 v50, 1.0, v50\n"
      "v_add_f32 v51, 1.0, v51\n"
      "v_rcp_f32 v48, v48\n"
      "v_rcp_f32 v49, v49\n"
      "v_rcp_f32 v50, v50\n"
      "v_rcp_f32 v51, v51\n"
      "v_mul_f32 v48, v48, v180\n"
      "v_mul_f32 v49, v49, v181\n"
      "v_mul_f32 v50, v50, v182\n"
      "v_mul_f32 v51, v51, v183\n"
      "v_cvt_pk_bf16_f32 v54, v48, v49\n"
      "v_cvt_pk_bf16_f32 v55, v50, v51\n"
      "buffer_store_dwordx2 v[54:55], v15, s[40:43], s63 offen sc1\n"
      "s_add_u32 s63, s63, 1024\n"
      "v_add_f32 v184, v184, v28\n"
      "v_add_f32 v185, v185, v29\n"
      "v_add_f32 v186, v186, v30\n"
      "v_add_f32 v187, v187, v31\n"
      "v_mul_f32 v44, v184, v184\n"
      "v_mul_f32 v45, v185, v185\n"
      "v_mul_f32 v46, v186, v186\n"
      "v_mul_f32 v47, v187, v187\n"
      "v_fma_f32 v44, v44, v20, s62\n"
      "v_fma_f32 v45, v45, v20, s62\n"
      "v_fma_f32 v46, v46, v20, s62\n"
      "v_fma_f32 v47, v47, v20, s62\n"
      "v_mul_f32 v44, v44, v184\n"
      "v_mul_f32 v45, v45, v185\n"
      "v_mul_f32 v46, v46, v186\n"
      "v_mul_f32 v47, v47, v187\n"
      "v_exp_f32 v44, v44\n"
      "v_exp_f32 v45, v45\n"
      "v_exp_f32 v46, v46\n"
      "v_exp_f32 v47, v47\n"
      "v_add_f32 v44, 1.0, v44\n"
      "v_add_f32 v45, 1.0, v45\n"
      "v_add_f32 v46, 1.0, v46\n"
      "v_add_f32 v47, 1.0, v47\n"
      "v_rcp_f32 v44, v44\n"
      "v_rcp_f32 v45, v45\n"
      "v_rcp_f32 v46, v46\n"
      "v_rcp_f32 v47, v47\n"
      "v_mul_f32 v44, v44, v184\n"
      "v_mul_f32 v45, v45, v185\n"
      "v_mul_f32 v46, v46, v186\n"
      "v_mul_f32 v47, v47, v187\n"
      "v_cvt_pk_bf16_f32 v56, v44, v45\n"
      "v_cvt_pk_bf16_f32 v57, v46, v47\n"
      "buffer_store_dwordx2 v[56:57], v15, s[40:43], s63 offen sc1\n"
      "s_add_u32 s63, s63, 1024\n"
      "v_add_f32 v188, v188, v28\n"
      "v_add_f32 v189, v189, v29\n"
      "v_add_f32 v190, v190, v30\n"
      "v_add_f32 v191, v191, v31\n"
      "v_mul_f32 v48, v188, v188\n"
      "v_mul_f32 v49, v189, v189\n"
      "v_mul_f32 v50, v190, v190\n"
      "v_mul_f32 v51, v191, v191\n"
      "v_fma_f32 v48, v48, v20, s62\n"
      "v_fma_f32 v49, v49, v20, s62\n"
      "v_fma_f32 v50, v50, v20, s62\n"
      "v_fma_f32 v51, v51, v20, s62\n"
      "v_mul_f32 v48, v48, v188\n"
      "v_mul_f32 v49, v49, v189\n"
      "v_mul_f32 v50, v50, v190\n"
      "v_mul_f32 v51, v51, v191\n"
      "v_exp_f32 v48, v48\n"
      "v_exp_f32 v49, v49\n"
      "v_exp_f32 v50, v50\n"
      "v_exp_f32 v51, v51\n"
      "v_add_f32 v48, 1.0, v48\n"
      "v_add_f32 v49, 1.0, v49\n"
      "v_add_f32 v50, 1.0, v50\n"
      "v_add_f32 v51, 1.0, v51\n"
      "v_rcp_f32 v48, v48\n"
      "v_rcp_f32 v49, v49\n"
      "v_rcp_f32 v50, v50\n"
      "v_rcp_f32 v51, v51\n"
      "v_mul_f32 v48, v48, v188\n"
      "v_mul_f32 v49, v49, v189\n"
      "v_mul_f32 v50, v50, v190\n"
      "v_mul_f32 v51, v51, v191\n"
      "v_cvt_pk_bf16_f32 v58, v48, v49\n"
      "v_cvt_pk_bf16_f32 v59, v50, v51\n"
      "buffer_store_dwordx2 v[58:59], v15, s[40:43], s63 offen sc1\n"
      "s_add_u32 s63, s63, 1024\n"
      "v_add_f32 v192, v192, v28\n"
      "v_add_f32 v193, v193, v29\n"
      "v_add_f32 v194, v194, v30\n"
      "v_add_f32 v195, v195, v31\n"
      "v_mul_f32 v44, v192, v192\n"
      "v_mul_f32 v45, v193, v193\n"
      "v_mul_f32 v46, v194, v194\n"
      "v_mul_f32 v47, v195, v195\n"
      "v_fma_f32 v44, v44, v20, s62\n"
      "v_fma_f32 v45, v45, v20, s62\n"
      "v_fma_f32 v46, v46, v20, s62\n"
      "v_fma_f32 v47, v47, v20, s62\n"
      "v_mul_f32 v44, v44, v192\n"
      "v_mul_f32 v45, v45, v193\n"
      "v_mul_f32 v46, v46, v194\n"
      "v_mul_f32 v47, v47, v195\n"
      "v_exp_f32 v44, v44\n"
      "v_exp_f32 v45, v45\n"
      "v_exp_f32 v46, v46\n"
      "v_exp_f32 v47, v47\n"
      "v_add_f32 v44, 1.0, v44\n"
      "v_add_f32 v45, 1.0, v45\n"
      "v_add_f32 v46, 1.0, v46\n"
      "v_add_f32 v47, 1.0, v47\n"
      "v_rcp_f32 v44, v44\n"
      "v_rcp_f32 v45, v45\n"
      "v_rcp_f32 v46, v46\n"
      "v_rcp_f32 v47, v47\n"
      "v_mul_f32 v44, v44, v192\n"
      "v_mul_f32 v45, v45, v193\n"
      "v_mul_f32 v46, v46, v194\n"
      "v_mul_f32 v47, v47, v195\n"
      "v_cvt_pk_bf16_f32 v52, v44, v45\n"
      "v_cvt_pk_bf16_f32 v53, v46, v47\n"
      "buffer_store_dwordx2 v[52:53], v15, s[40:43], s63 offen sc1\n"
      "s_add_u32 s63, s63, 1024\n"
      "v_add_f32 v196, v196, v28\n"
      "v_add_f32 v197, v197, v29\n"
      "v_add_f32 v198, v198, v30\n"
      "v_add_f32 v199, v199, v31\n"
      "v_mul_f32 v48, v196, v196\n"
      "v_mul_f32 v49, v197, v197\n"
      "v_mul_f32 v50, v198, v198\n"
      "v_mul_f32 v51, v199, v199\n"
      "v_fma_f32 v48, v48, v20, s62\n"
      "v_fma_f32 v49, v49, v20, s62\n"
      "v_fma_f32 v50, v50, v20, s62\n"
      "v_fma_f32 v51, v51, v20, s62\n"
      "v_mul_f32 v48, v48, v196\n"
      "v_mul_f32 v49, v49, v197\n"
      "v_mul_f32 v50, v50, v198\n"
      "v_mul_f32 v51, v51, v199\n"
      "v_exp_f32 v48, v48\n"
      "v_exp_f32 v49, v49\n"
      "v_exp_f32 v50, v50\n"
      "v_exp_f32 v51, v51\n"
      "v_add_f32 v48, 1.0, v48\n"
      "v_add_f32 v49, 1.0, v49\n"
      "v_add_f32 v50, 1.0, v50\n"
      "v_add_f32 v51, 1.0, v51\n"
      "v_rcp_f32 v48, v48\n"
      "v_rcp_f32 v49, v49\n"
      "v_rcp_f32 v50, v50\n"
      "v_rcp_f32 v51, v51\n"
      "v_mul_f32 v48, v48, v196\n"
      "v_mul_f32 v49, v49, v197\n"
      "v_mul_f32 v50, v50, v198\n"
      "v_mul_f32 v51, v51, v199\n"
      "v_cvt_pk_bf16_f32 v54, v48, v49\n"
      "v_cvt_pk_bf16_f32 v55, v50, v51\n"
      "buffer_store_dwordx2 v[54:55], v15, s[40:43], s63 offen sc1\n"
      "s_add_u32 s63, s63, 1024\n"
      "s_mov_b32 s63, 36864\n"
      "v_add_f32 v200, v200, v32\n"
      "v_add_f32 v201, v201, v33\n"
      "v_add_f32 v202, v202, v34\n"
      "v_add_f32 v203, v203, v35\n"
      "v_mul_f32 v44, v200, v200\n"
      "v_mul_f32 v45, v201, v201\n"
      "v_mul_f32 v46, v202, v202\n"
      "v_mul_f32 v47, v203, v203\n"
      "v_fma_f32 v44, v44, v20, s62\n"
      "v_fma_f32 v45, v45, v20, s62\n"
      "v_fma_f32 v46, v46, v20, s62\n"
      "v_fma_f32 v47, v47, v20, s62\n"
      "v_mul_f32 v44, v44, v200\n"
      "v_mul_f32 v45, v45, v201\n"
      "v_mul_f32 v46, v46, v202\n"
      "v_mul_f32 v47, v47, v203\n"
      "v_exp_f32 v44, v44\n"
      "v_exp_f32 v45, v45\n"
      "v_exp_f32 v46, v46\n"
      "v_exp_f32 v47, v47\n"
      "v_add_f32 v44, 1.0, v44\n"
      "v_add_f32 v45, 1.0, v45\n"
      "v_add_f32 v46, 1.0, v46\n"
      "v_add_f32 v47, 1.0, v47\n"
      "v_rcp_f32 v44, v44\n"
      "v_rcp_f32 v45, v45\n"
      "v_rcp_f32 v46, v46\n"
      "v_rcp_f32 v47, v47\n"
      "v_mul_f32 v44, v44, v200\n"
      "v_mul_f32 v45, v45, v201\n"
      "v_mul_f32 v46, v46, v202\n"
      "v_mul_f32 v47, v47, v203\n"
      "v_cvt_pk_bf16_f32 v56, v44, v45\n"
      "v_cvt_pk_bf16_f32 v57, v46, v47\n"
      "buffer_store_dwordx2 v[56:57], v15, s[40:43], s63 offen sc1\n"
      "s_add_u32 s63, s63, 1024\n"
      "v_add_f32 v204, v204, v32\n"
      "v_add_f32 v205, v205, v33\n"
      "v_add_f32 v206, v206, v34\n"
      "v_add_f32 v207, v207, v35\n"
      "v_mul_f32 v48, v204, v204\n"
      "v_mul_f32 v49, v205, v205\n"
      "v_mul_f32 v50, v206, v206\n"
      "v_mul_f32 v51, v207, v207\n"
      "v_fma_f32 v48, v48, v20, s62\n"
      "v_fma_f32 v49, v49, v20, s62\n"
      "v_fma_f32 v50, v50, v20, s62\n"
      "v_fma_f32 v51, v51, v20, s62\n"
      "v_mul_f32 v48, v48, v204\n"
      "v_mul_f32 v49, v49, v205\n"
      "v_mul_f32 v50, v50, v206\n"
      "v_mul_f32 v51, v51, v207\n"
      "v_exp_f32 v48, v48\n"
      "v_exp_f32 v49, v49\n"
      "v_exp_f32 v50, v50\n"
      "v_exp_f32 v51, v51\n"
      "v_add_f32 v48, 1.0, v48\n"
      "v_add_f32 v49, 1.0, v49\n"
      "v_add_f32 v50, 1.0, v50\n"
      "v_add_f32 v51, 1.0, v51\n"
      "v_rcp_f32 v48, v48\n"
      "v_rcp_f32 v49, v49\n"
      "v_rcp_f32 v50, v50\n"
      "v_rcp_f32 v51, v51\n"
      "v_mul_f32 v48, v48, v204\n"
      "v_mul_f32 v49, v49, v205\n"
      "v_mul_f32 v50, v50, v206\n"
      "v_mul_f32 v51, v51, v207\n"
      "v_cvt_pk_bf16_f32 v58, v48, v49\n"
      "v_cvt_pk_bf16_f32 v59, v50, v51\n"
      "buffer_store_dwordx2 v[58:59], v15, s[40:43], s63 offen sc1\n"
      "s_add_u32 s63, s63, 1024\n"
      "v_add_f32 v208, v208, v32\n"
      "v_add_f32 v209, v209, v33\n"
      "v_add_f32 v210, v210, v34\n"
      "v_add_f32 v211, v211, v35\n"
      "v_mul_f32 v44, v208, v208\n"
      "v_mul_f32 v45, v209, v209\n"
      "v_mul_f32 v46, v210, v210\n"
      "v_mul_f32 v47, v211, v211\n"
      "v_fma_f32 v44, v44, v20, s62\n"
      "v_fma_f32 v45, v45, v20, s62\n"
      "v_fma_f32 v46, v46, v20, s62\n"
      "v_fma_f32 v47, v47, v20, s62\n"
      "v_mul_f32 v44, v44, v208\n"
      "v_mul_f32 v45, v45, v209\n"
      "v_mul_f32 v46, v46, v210\n"
      "v_mul_f32 v47, v47, v211\n"
      "v_exp_f32 v44, v44\n"
      "v_exp_f32 v45, v45\n"
      "v_exp_f32 v46, v46\n"
      "v_exp_f32 v47, v47\n"
      "v_add_f32 v44, 1.0, v44\n"
      "v_add_f32 v45, 1.0, v45\n"
      "v_add_f32 v46, 1.0, v46\n"
      "v_add_f32 v47, 1.0, v47\n"
      "v_rcp_f32 v44, v44\n"
      "v_rcp_f32 v45, v45\n"
      "v_rcp_f32 v46, v46\n"
      "v_rcp_f32 v47, v47\n"
      "v_mul_f32 v44, v44, v208\n"
      "v_mul_f32 v45, v45, v209\n"
      "v_mul_f32 v46, v46, v210\n"
      "v_mul_f32 v47, v47, v211\n"
      "v_cvt_pk_bf16_f32 v52, v44, v45\n"
      "v_cvt_pk_bf16_f32 v53, v46, v47\n"
      "buffer_store_dwordx2 v[52:53], v15, s[40:43], s63 offen sc1\n"
      "s_add_u32 s63, s63, 1024\n"
      "v_add_f32 v212, v212, v32\n"
      "v_add_f32 v213, v213, v33\n"
      "v_add_f32 v214, v214, v34\n"
      "v_add_f32 v215, v215, v35\n"
      "v_mul_f32 v48, v212, v212\n"
      "v_mul_f32 v49, v213, v213\n"
      "v_mul_f32 v50, v214, v214\n"
      "v_mul_f32 v51, v215, v215\n"
      "v_fma_f32 v48, v48, v20, s62\n"
      "v_fma_f32 v49, v49, v20, s62\n"
      "v_fma_f32 v50, v50, v20, s62\n"
      "v_fma_f32 v51, v51, v20, s62\n"
      "v_mul_f32 v48, v48, v212\n"
      "v_mul_f32 v49, v49, v213\n"
      "v_mul_f32 v50, v50, v214\n"
      "v_mul_f32 v51, v51, v215\n"
      "v_exp_f32 v48, v48\n"
      "v_exp_f32 v49, v49\n"
      "v_exp_f32 v50, v50\n"
      "v_exp_f32 v51, v51\n"
      "v_add_f32 v48, 1.0, v48\n"
      "v_add_f32 v49, 1.0, v49\n"
      "v_add_f32 v50, 1.0, v50\n"
      "v_add_f32 v51, 1.0, v51\n"
      "v_rcp_f32 v48, v48\n"
      "v_rcp_f32 v49, v49\n"
      "v_rcp_f32 v50, v50\n"
      "v_rcp_f32 v51, v51\n"
      "v_mul_f32 v48, v48, v212\n"
      "v_mul_f32 v49, v49, v213\n"
      "v_mul_f32 v50, v50, v214\n"
      "v_mul_f32 v51, v51, v215\n"
      "v_cvt_pk_bf16_f32 v54, v48, v49\n"
      "v_cvt_pk_bf16_f32 v55, v50, v51\n"
      "buffer_store_dwordx2 v[54:55], v15, s[40:43], s63 offen sc1\n"
      "s_add_u32 s63, s63, 1024\n"
      "v_add_f32 v216, v216, v32\n"
      "v_add_f32 v217, v217, v33\n"
      "v_add_f32 v218, v218, v34\n"
      "v_add_f32 v219, v219, v35\n"
      "v_mul_f32 v44, v216, v216\n"
      "v_mul_f32 v45, v217, v217\n"
      "v_mul_f32 v46, v218, v218\n"
      "v_mul_f32 v47, v219, v219\n"
      "v_fma_f32 v44, v44, v20, s62\n"
      "v_fma_f32 v45, v45, v20, s62\n"
      "v_fma_f32 v46, v46, v20, s62\n"
      "v_fma_f32 v47, v47, v20, s62\n"
      "v_mul_f32 v44, v44, v216\n"
      "v_mul_f32 v45, v45, v217\n"
      "v_mul_f32 v46, v46, v218\n"
      "v_mul_f32 v47, v47, v219\n"
      "v_exp_f32 v44, v44\n"
      "v_exp_f32 v45, v45\n"
      "v_exp_f32 v46, v46\n"
      "v_exp_f32 v47, v47\n"
      "v_add_f32 v44, 1.0, v44\n"
      "v_add_f32 v45, 1.0, v45\n"
      "v_add_f32 v46, 1.0, v46\n"
      "v_add_f32 v47, 1.0, v47\n"
      "v_rcp_f32 v44, v44\n"
      "v_rcp_f32 v45, v45\n"
      "v_rcp_f32 v46, v46\n"
      "v_rcp_f32 v47, v47\n"
      "v_mul_f32 v44, v44, v216\n"
      "v_mul_f32 v45, v45, v217\n"
      "v_mul_f32 v46, v46, v218\n"
      "v_mul_f32 v47, v47, v219\n"
      "v_cvt_pk_bf16_f32 v56, v44, v45\n"
      "v_cvt_pk_bf16_f32 v57, v46, v47\n"
      "buffer_store_dwordx2 v[56:57], v15, s[40:43], s63 offen sc1\n"
      "s_add_u32 s63, s63, 1024\n"
      "v_add_f32 v220, v220, v32\n"
      "v_add_f32 v221, v221, v33\n"
      "v_add_f32 v222, v222, v34\n"
      "v_add_f32 v223, v223, v35\n"
      "v_mul_f32 v48, v220, v220\n"
      "v_mul_f32 v49, v221, v221\n"
      "v_mul_f32 v50, v222, v222\n"
      "v_mul_f32 v51, v223, v223\n"
      "v_fma_f32 v48, v48, v20, s62\n"
      "v_fma_f32 v49, v49, v20, s62\n"
      "v_fma_f32 v50, v50, v20, s62\n"
      "v_fma_f32 v51, v51, v20, s62\n"
      "v_mul_f32 v48, v48, v220\n"
      "v_mul_f32 v49, v49, v221\n"
      "v_mul_f32 v50, v50, v222\n"
      "v_mul_f32 v51, v51, v223\n"
      "v_exp_f32 v48, v48\n"
      "v_exp_f32 v49, v49\n"
      "v_exp_f32 v50, v50\n"
      "v_exp_f32 v51, v51\n"
      "v_add_f32 v48, 1.0, v48\n"
      "v_add_f32 v49, 1.0, v49\n"
      "v_add_f32 v50, 1.0, v50\n"
      "v_add_f32 v51, 1.0, v51\n"
      "v_rcp_f32 v48, v48\n"
      "v_rcp_f32 v49, v49\n"
      "v_rcp_f32 v50, v50\n"
      "v_rcp_f32 v51, v51\n"
      "v_mul_f32 v48, v48, v220\n"
      "v_mul_f32 v49, v49, v221\n"
      "v_mul_f32 v50, v50, v222\n"
      "v_mul_f32 v51, v51, v223\n"
      "v_cvt_pk_bf16_f32 v58, v48, v49\n"
      "v_cvt_pk_bf16_f32 v59, v50, v51\n"
      "buffer_store_dwordx2 v[58:59], v15, s[40:43], s63 offen sc1\n"
      "s_add_u32 s63, s63, 1024\n"
      "v_add_f32 v224, v224, v32\n"
      "v_add_f32 v225, v225, v33\n"
      "v_add_f32 v226, v226, v34\n"
      "v_add_f32 v227, v227, v35\n"
      "v_mul_f32 v44, v224, v224\n"
      "v_mul_f32 v45, v225, v225\n"
      "v_mul_f32 v46, v226, v226\n"
      "v_mul_f32 v47, v227, v227\n"
      "v_fma_f32 v44, v44, v20, s62\n"
      "v_fma_f32 v45, v45, v20, s62\n"
      "v_fma_f32 v46, v46, v20, s62\n"
      "v_fma_f32 v47, v47, v20, s62\n"
      "v_mul_f32 v44, v44, v224\n"
      "v_mul_f32 v45, v45, v225\n"
      "v_mul_f32 v46, v46, v226\n"
      "v_mul_f32 v47, v47, v227\n"
      "v_exp_f32 v44, v44\n"
      "v_exp_f32 v45, v45\n"
      "v_exp_f32 v46, v46\n"
      "v_exp_f32 v47, v47\n"
      "v_add_f32 v44, 1.0, v44\n"
      "v_add_f32 v45, 1.0, v45\n"
      "v_add_f32 v46, 1.0, v46\n"
      "v_add_f32 v47, 1.0, v47\n"
      "v_rcp_f32 v44, v44\n"
      "v_rcp_f32 v45, v45\n"
      "v_rcp_f32 v46, v46\n"
      "v_rcp_f32 v47, v47\n"
      "v_mul_f32 v44, v44, v224\n"
      "v_mul_f32 v45, v45, v225\n"
      "v_mul_f32 v46, v46, v226\n"
      "v_mul_f32 v47, v47, v227\n"
      "v_cvt_pk_bf16_f32 v52, v44, v45\n"
      "v_cvt_pk_bf16_f32 v53, v46, v47\n"
      "buffer_store_dwordx2 v[52:53], v15, s[40:43], s63 offen sc1\n"
      "s_add_u32 s63, s63, 1024\n"
      "v_add_f32 v228, v228, v32\n"
      "v_add_f32 v229, v229, v33\n"
      "v_add_f32 v230, v230, v34\n"
      "v_add_f32 v231, v231, v35\n"
      "v_mul_f32 v48, v228, v228\n"
      "v_mul_f32 v49, v229, v229\n"
      "v_mul_f32 v50, v230, v230\n"
      "v_mul_f32 v51, v231, v231\n"
      "v_fma_f32 v48, v48, v20, s62\n"
      "v_fma_f32 v49, v49, v20, s62\n"
      "v_fma_f32 v50, v50, v20, s62\n"
      "v_fma_f32 v51, v51, v20, s62\n"
      "v_mul_f32 v48, v48, v228\n"
      "v_mul_f32 v49, v49, v229\n"
      "v_mul_f32 v50, v50, v230\n"
      "v_mul_f32 v51, v51, v231\n"
      "v_exp_f32 v48, v48\n"
      "v_exp_f32 v49, v49\n"
      "v_exp_f32 v50, v50\n"
      "v_exp_f32 v51, v51\n"
      "v_add_f32 v48, 1.0, v48\n"
      "v_add_f32 v49, 1.0, v49\n"
      "v_add_f32 v50, 1.0, v50\n"
      "v_add_f32 v51, 1.0, v51\n"
      "v_rcp_f32 v48, v48\n"
      "v_rcp_f32 v49, v49\n"
      "v_rcp_f32 v50, v50\n"
      "v_rcp_f32 v51, v51\n"
      "v_mul_f32 v48, v48, v228\n"
      "v_mul_f32 v49, v49, v229\n"
      "v_mul_f32 v50, v50, v230\n"
      "v_mul_f32 v51, v51, v231\n"
      "v_cvt_pk_bf16_f32 v54, v48, v49\n"
      "v_cvt_pk_bf16_f32 v55, v50, v51\n"
      "buffer_store_dwordx2 v[54:55], v15, s[40:43], s63 offen sc1\n"
      "s_add_u32 s63, s63, 1024\n"
      "v_add_f32 v232, v232, v32\n"
      "v_add_f32 v233, v233, v33\n"
      "v_add_f32 v234, v234, v34\n"
      "v_add_f32 v235, v235, v35\n"
      "v_mul_f32 v44, v232, v232\n"
      "v_mul_f32 v45, v233, v233\n"
      "v_mul_f32 v46, v234, v234\n"
      "v_mul_f32 v47, v235, v235\n"
      "v_fma_f32 v44, v44, v20, s62\n"
      "v_fma_f32 v45, v45, v20, s62\n"
      "v_fma_f32 v46, v46, v20, s62\n"
      "v_fma_f32 v47, v47, v20, s62\n"
      "v_mul_f32 v44, v44, v232\n"
      "v_mul_f32 v45, v45, v233\n"
      "v_mul_f32 v46, v46, v234\n"
      "v_mul_f32 v47, v47, v235\n"
      "v_exp_f32 v44, v44\n"
      "v_exp_f32 v45, v45\n"
      "v_exp_f32 v46, v46\n"
      "v_exp_f32 v47, v47\n"
      "v_add_f32 v44, 1.0, v44\n"
      "v_add_f32 v45, 1.0, v45\n"
      "v_add_f32 v46, 1.0, v46\n"
      "v_add_f32 v47, 1.0, v47\n"
      "v_rcp_f32 v44, v44\n"
      "v_rcp_f32 v45, v45\n"
      "v_rcp_f32 v46, v46\n"
      "v_rcp_f32 v47, v47\n"
      "v_mul_f32 v44, v44, v232\n"
      "v_mul_f32 v45, v45, v233\n"
      "v_mul_f32 v46, v46, v234\n"
      "v_mul_f32 v47, v47, v235\n"
      "v_cvt_pk_bf16_f32 v56, v44, v45\n"
      "v_cvt_pk_bf16_f32 v57, v46, v47\n"
      "buffer_store_dwordx2 v[56:57], v15, s[40:43], s63 offen sc1\n"
      "s_add_u32 s63, s63, 1024\n"
      "s_waitcnt vmcnt(0)\n"
      : "+{v0}"(tid) : [b1lo] "s"(lo32(p.b1)), [b1hi] "s"(hi32(p.b1)) : "memory", "vcc", "scc", CLV4, CLS);
    farb = (__builtin_amdgcn_readfirstlane(wait_ge(ws, PARW, 1, 0, tid & 63, budget)) & 8192) ^ 8192;
    copy_w2();
    publish(ws, G1F + b * 128, tid, 1);
  }
  {
    const int l = tid & 63, w = tid >> 6, c = l & 15, g = l >> 4, q = (l >> 2) & 3, pp = l & 3;
    const int st = b / 6, dt = b % 6;
    TAB(TBG, 0) = (2 * w + (g >> 1)) * 16384 + (g & 1) * 4096 + st * 256 + (((c >> 1) ^ (4 * (w & 1) + g)) * 2 + (c & 1)) * 16;
#pragma unroll
    for (int j = 0; j < 3; ++j) { const int i = 4 * j + w; TAB(TBG, 1 + j) = (i / 6) * 36864 + (6 * dt + i % 6) * 1024 + l * 16; }
#pragma unroll
    for (int j = 0; j < 2; ++j) TAB(TBG, 4 + j) = (4 * g + q) * 256 + ((2 * w + j) ^ (4 * (g & 1) + q)) * 32 + pp * 8;
    TAB(TBG, 6) = l * 8;
    TAB(TBG, 7) = (128 * st + 32 * w + c) * 2;
    TAB(TBG, 8) = YB + ((4 * st + w) * 36 + 3 * dt) * 1024 + l * 8;
    TAB(TBG, 11) = lo32(p.ws); TAB(TBG, 12) = hi32(p.ws);
    TAB(TBG, 13) = farb; TAB(TBG, 14) = 0; TAB(TBG, 15) = w * 1024;
    if (w < 2) wait_ge(ws, G1F + w * 6144, 48, 1, l, budget);
    __syncthreads();
    asm volatile("s_waitcnt lgkmcnt(0)\n"
      "v_lshlrev_b32 v4, 2, v0\n"
      "v_add_u32 v4, 147456, v4\n"
      "ds_read_b32 v5, v4 offset:0\n"
      "ds_read_b32 v6, v4 offset:1024\n"
      "ds_read_b32 v7, v4 offset:2048\n"
      "ds_read_b32 v8, v4 offset:3072\n"
      "ds_read_b32 v9, v4 offset:4096\n"
      "ds_read_b32 v10, v4 offset:5120\n"
      "ds_read_b32 v11, v4 offset:6144\n"
      "ds_read_b32 v12, v4 offset:7168\n"
      "ds_read_b32 v13, v4 offset:8192\n"
      "s_waitcnt lgkmcnt(0)\n"
      "ds_read_b32 v24, v4 offset:11264\n"
      "ds_read_b32 v25, v4 offset:12288\n"
      "ds_read_b32 v26, v4 offset:13312\n"
      "ds_read_b32 v27, v4 offset:14336\n"
      "ds_read_b32 v28, v4 offset:15360\n"
      "s_waitcnt lgkmcnt(0)\n"
      "v_readfirstlane_b32 s40, v24\n"
      "v_readfirstlane_b32 s41, v25\n"
      "v_readfirstlane_b32 s44, v26\n"
      "v_readfirstlane_b32 s45, v27\n"
      "v_readfirstlane_b32 s36, v28\n"
      "s_nop 4\n"
      "s_mov_b32 s42, 226492416\n"
      "s_mov_b32 s43, 0x20000\n"
      "s_mov_b32 s46, 18874368\n"
      "s_mov_b32 s47, 0x20000\n"
      "s_mov_b32 s53, 7340032\n"
      "s_mov_b32 s51, 16777216\n"
      "s_or_b32 s50, s51, s44\n"
      "s_mov_b32 s54, 0\n"
      "s_mov_b32 s55, 0\n"
      "s_mov_b32 s37, 0\n"
      "v_mov_b32 v160, 0\n"
      "v_mov_b32 v161, 0\n"
      "v_mov_b32 v162, 0\n"
      "v_mov_b32 v163, 0\n"
      "v_mov_b32 v164, 0\n"
      "v_mov_b32 v165, 0\n"
      "v_mov_b32 v166, 0\n"
      "v_mov_b32 v167, 0\n"
      "v_mov_b32 v168, 0\n"
      "v_mov_b32 v169, 0\n"
      "v_mov_b32 v170, 0\n"
      "v_mov_b32 v171, 0\n"
      "v_mov_b32 v172, 0\n"
      "v_mov_b32 v173, 0\n"
      "v_mov_b32 v174, 0\n"
      "v_mov_b32 v175, 0\n"
      "v_mov_b32 v176, 0\n"
      "v_mov_b32 v177, 0\n"
      "v_mov_b32 v178, 0\n"
      "v_mov_b32 v179, 0\n"
      "v_mov_b32 v180, 0\n"
      "v_mov_b32 v181, 0\n"
      "v_mov_b32 v182, 0\n"
      "v_mov_b32 v183, 0\n"
      "v_mov_b32 v184, 0\n"
      "v_mov_b32 v185, 0\n"
      "v_mov_b32 v186, 0\n"
      "v_mov_b32 v187, 0\n"
      "v_mov_b32 v188, 0\n"
      "v_mov_b32 v189, 0\n"
      "v_mov_b32 v190, 0\n"
      "v_mov_b32 v191, 0\n"
      "v_mov_b32 v192, 0\n"
      "v_mov_b32 v193, 0\n"
      "v_mov_b32 v194, 0\n"
      "v_mov_b32 v195, 0\n"
      "v_mov_b32 v196, 0\n"
      "v_mov_b32 v197, 0\n"
      "v_mov_b32 v198, 0\n"
      "v_mov_b32 v199, 0\n"
      "v_mov_b32 v200, 0\n"
      "v_mov_b32 v201, 0\n"
      "v_mov_b32 v202, 0\n"
      "v_mov_b32 v203, 0\n"
      "v_mov_b32 v204, 0\n"
      "v_mov_b32 v205, 0\n"
      "v_mov_b32 v206, 0\n"
      "v_mov_b32 v207, 0\n"
      "s_add_u32 s58, s54, s36\n"
      "s_add_u32 m0, s58, 0\n"
      "s_add_u32 s38, s50, 0\n"
      "buffer_load_dwordx4 v5, s[40:43], s38 offen sc1 lds\n"
      "s_add_u32 m0, s58, 4096\n"
      "s_add_u32 s38, s50, 131072\n"
      "buffer_load_dwordx4 v5, s[40:43], s38 offen sc1 lds\n"
      "s_add_u32 m0, s58, 8192\n"
      "s_add_u32 s38, s50, 262144\n"
      "buffer_load_dwordx4 v5, s[40:43], s38 offen sc1 lds\n"
      "s_add_u32 m0, s58, 12288\n"
      "s_add_u32 s38, s50, 393216\n"
      "buffer_load_dwordx4 v5, s[40:43], s38 offen sc1 lds\n"
      "s_add_u32 m0, s58, 16384\n"
      "s_nop 0\n"
      "buffer_load_dwordx4 v6, s[40:43], s53 offen sc1 lds\n"
      "s_add_u32 s51, s51, 524288\n"
      "s_min_u32 s51, s51, 54001664\n"
      "s_lshr_b32 s52, s51, 8\n"
      "s_add_u32 m0, s58, 20480\n"
      "s_nop 0\n"
      "buffer_load_dwordx4 v7, s[40:43], s53 offen sc1 lds\n"
      "s_xor_b32 s52, s52, s44\n"
      "s_and_b32 s52, s52, 8192\n"
      "s_or_b32 s50, s51, s52\n"
      "s_add_u32 m0, s58, 24576\n"
      "s_nop 0\n"
      "buffer_load_dwordx4 v8, s[40:43], s53 offen sc1 lds\n"
      "s_add_u32 s53, s53, 73728\n"
      "s_add_u32 s54, s54, 28672\n"
      "s_cmp_eq_u32 s54, 143360\n"
      "s_cselect_b32 s54, 0, s54\n"
      "s_add_u32 s58, s54, s36\n"
      "s_add_u32 m0, s58, 0\n"
      "s_add_u32 s38, s50, 0\n"
      "buffer_load_dwordx4 v5, s[40:43], s38 offen sc1 lds\n"
      "s_add_u32 m0, s58, 4096\n"
      "s_add_u32 s38, s50, 131072\n"
      "buffer_load_dwordx4 v5, s[40:43], s38 offen sc1 lds\n"
      "s_add_u32 m0, s58, 8192\n"
      "s_add_u32 s38, s50, 262144\n"
      "buffer_load_dwordx4 v5, s[40:43], s38 offen sc1 lds\n"
      "s_add_u32 m0, s58, 12288\n"
      "s_add_u32 s38, s50, 393216\n"
      "buffer_load_dwordx4 v5, s[40:43], s38 offen sc1 lds\n"
      "s_add_u32 m0, s58, 16384\n"
      "s_nop 0\n"
      "buffer_load_dwordx4 v6, s[40:43], s53 offen sc1 lds\n"
      "s_add_u32 s51, s51, 524288\n"
      "s_min_u32 s51, s51, 54001664\n"
      "s_lshr_b32 s52, s51, 8\n"
      "s_add_u32 m0, s58, 20480\n"
      "s_nop 0\n"
      "buffer_load_dwordx4 v7, s[40:43], s53 offen sc1 lds\n"
      "s_xor_b32 s52, s52, s44\n"
      "s_and_b32 s52, s52, 8192\n"
      "s_or_b32 s50, s51, s52\n"
      "s_add_u32 m0, s58, 24576\n"
      "s_nop 0\n"
      "buffer_load_dwordx4 v8, s[40:43], s53 offen sc1 lds\n"
      "s_add_u32 s53, s53, 73728\n"
      "s_add_u32 s54, s54, 28672\n"
      "s_cmp_eq_u32 s54, 143360\n"
      "s_cselect_b32 s54, 0, s54\n"
      "s_add_u32 s58, s54, s36\n"
      "s_add_u32 m0, s58, 0\n"
      "s_add_u32 s38, s50, 0\n"
      "buffer_load_dwordx4 v5, s[40:43], s38 offen sc1 lds\n"
      "s_add_u32 m0, s58, 4096\n"
      "s_add_u32 s38, s50, 131072\n"
      "buffer_load_dwordx4 v5, s[40:43], s38 offen sc1 lds\n"
      "s_add_u32 m0, s58, 8192\n"
      "s_add_u32 s38, s50, 262144\n"
      "buffer_load_dwordx4 v5, s[40:43], s38 offen sc1 lds\n"
      "s_add_u32 m0, s58, 12288\n"
      "s_add_u32 s38, s50, 393216\n"
      "buffer_load_dwordx4 v5, s[40:43], s38 offen sc1 lds\n"
      "s_add_u32 m0, s58, 16384\n"
      "s_nop 0\n"
      "buffer_load_dwordx4 v6, s[40:43], s53 offen sc1 lds\n"
      "s_add_u32 s51, s51, 524288\n"
      "s_min_u32 s51, s51, 54001664\n"
      "s_lshr_b32 s52, s51, 8\n"
      "s_add_u32 m0, s58, 20480\n"
      "s_nop 0\n"
      "buffer_load_dwordx4 v7, s[40:43], s53 offen sc1 lds\n"
      "s_xor_b32 s52, s52, s44\n"
      "s_and_b32 s52, s52, 8192\n"
      "s_or_b32 s50, s51, s52\n"
      "s_add_u32 m0, s58, 24576\n"
      "s_nop 0\n"
      "buffer_load_dwordx4 v8, s[40:43], s53 offen sc1 lds\n"
      "s_add_u32 s53, s53, 73728\n"
      "s_add_u32 s54, s54, 28672\n"
      "s_cmp_eq_u32 s54, 143360\n"
      "s_cselect_b32 s54, 0, s54\n"
      "s_add_u32 s58, s54, s36\n"
      "s_add_u32 m0, s58, 0\n"
      "s_add_u32 s38, s50, 0\n"
      "buffer_load_dwordx4 v5, s[40:43], s38 offen sc1 lds\n"
      "s_add_u32 m0, s58, 4096\n"
      "s_add_u32 s38, s50, 131072\n"
      "buffer_load_dwordx4 v5, s[40:43], s38 offen sc1 lds\n"
      "s_add_u32 m0, s58, 8192\n"
      "s_add_u32 s38, s50, 262144\n"
      "buffer_load_dwordx4 v5, s[40:43], s38 offen sc1 lds\n"
      "s_add_u32 m0, s58, 12288\n"
      "s_add_u32 s38, s50, 393216\n"
      "buffer_load_dwordx4 v5, s[40:43], s38 offen sc1 lds\n"
      "s_add_u32 m0, s58, 16384\n"
      "s_nop 0\n"
      "buffer_load_dwordx4 v6, s[40:43], s53 offen sc1 lds\n"
      "s_add_u32 s51, s51, 524288\n"
      "s_min_u32 s51, s51, 54001664\n"
      "s_lshr_b32 s52, s51, 8\n"
      "s_add_u32 m0, s58, 20480\n"
      "s_nop 0\n"
      "buffer_load_dwordx4 v7, s[40:43], s53 offen sc1 lds\n"
      "s_xor_b32 s52, s52, s44\n"
      "s_and_b32 s52, s52, 8192\n"
      "s_or_b32 s50, s51, s52\n"
      "s_add_u32 m0, s58, 24576\n"
      "s_nop 0\n"
      "buffer_load_dwordx4 v8, s[40:43], s53 offen sc1 lds\n"
      "s_add_u32 s53, s53, 73728\n"
      "s_add_u32 s54, s54, 28672\n"
      "s_cmp_eq_u32 s54, 143360\n"
      "s_cselect_b32 s54, 0, s54\n"
      "s_add_u32 s58, s54, s36\n"
      "s_add_u32 m0, s58, 0\n"
      "s_add_u32 s38, s50, 0\n"
      "buffer_load_dwordx4 v5, s[40:43], s38 offen sc1 lds\n"
      "s_add_u32 m0, s58, 4096\n"
      "s_add_u32 s38, s50, 131072\n"
      "buffer_load_dwordx4 v5, s[40:43], s38 offen sc1 lds\n"
      "s_add_u32 m0, s58, 8192\n"
      "s_add_u32 s38, s50, 262144\n"
      "buffer_load_dwordx4 v5, s[40:43], s38 offen sc1 lds\n"
      "s_add_u32 m0, s58, 12288\n"
      "s_add_u32 s38, s50, 393216\n"
      "buffer_load_dwordx4 v5, s[40:43], s38 offen sc1 lds\n"
      "s_add_u32 m0, s58, 16384\n"
      "s_nop 0\n"
      "buffer_load_dwordx4 v6, s[40:43], s53 offen sc1 lds\n"
      "s_add_u32 s51, s51, 524288\n"
      "s_min_u32 s51, s51, 54001664\n"
      "s_lshr_b32 s52, s51, 8\n"
      "s_add_u32 m0, s58, 20480\n"
      "s_nop 0\n"
      "buffer_load_dwordx4 v7, s[40:43], s53 offen sc1 lds\n"
      "s_xor_b32 s52, s52, s44\n"
      "s_and_b32 s52, s52, 8192\n"
      "s_or_b32 s50, s51, s52\n"
      "s_add_u32 m0, s58, 24576\n"
      "s_nop 0\n"
      "buffer_load_dwordx4 v8, s[40:43], s53 offen sc1 lds\n"
      "s_add_u32 s53, s53, 73728\n"
      "s_add_u32 s54, s54, 28672\n"
      "s_cmp_eq_u32 s54, 143360\n"
      "s_cselect_b32 s54, 0, s54\n"
      "s_waitcnt vmcnt(28)\n"
      "s_barrier\n"
      "v_add_u32 v16, s55, v9\n"
      "v_add_u32 v17, s55, v10\n"
      "v_add_u32 v19, s55, v11\n"
      "s_add_u32 s55, s55, 28672\n"
      "s_cmp_eq_u32 s55, 143360\n"
      "s_cselect_b32 s55, 0, s55\n"
      "ds_read_b64_tr_b16 v[24:25], v16 offset:0\n"
      "ds_read_b64_tr_b16 v[26:27], v16 offset:4096\n"
      "ds_read_b64_tr_b16 v[28:29], v17 offset:0\n"
      "ds_read_b64_tr_b16 v[30:31], v17 offset:4096\n"
      "ds_read_b64 v[40:41], v19 offset:16384\n"
      "ds_read_b64 v[42:43], v19 offset:16896\n"
      "ds_read_b64 v[44:45], v19 offset:17408\n"
      "ds_read_b64 v[46:47], v19 offset:17920\n"
      "ds_read_b64 v[48:49], v19 offset:18432\n"
      "ds_read_b64 v[50:51], v19 offset:18944\n"
      "ds_read_b64 v[52:53], v19 offset:19456\n"
      "ds_read_b64 v[54:55], v19 offset:19968\n"
      "ds_read_b64 v[56:57], v19 offset:20480\n"
      "ds_read_b64 v[58:59], v19 offset:20992\n"
      "ds_read_b64 v[60:61], v19 offset:21504\n"
      "ds_read_b64 v[62:63], v19 offset:22016\n"
      "ds_read_b64_tr_b16 v[32:33], v16 offset:8192\n"
      "ds_read_b64_tr_b16 v[34:35], v16 offset:12288\n"
      "ds_read_b64_tr_b16 v[36:37], v17 offset:8192\n"
      "ds_read_b64_tr_b16 v[38:39], v17 offset:12288\n"
      "ds_read_b64 v[64:65], v19 offset:22528\n"
      "ds_read_b64 v[66:67], v19 offset:23040\n"
      "ds_read_b64 v[68:69], v19 offset:23552\n"
      "ds_read_b64 v[70:71], v19 offset:24064\n"
      "ds_read_b64 v[72:73], v19 offset:24576\n"
      "ds_read_b64 v[74:75], v19 offset:25088\n"
      "ds_read_b64 v[76:77], v19 offset:25600\n"
      "ds_read_b64 v[78:79], v19 offset:26112\n"
      "ds_read_b64 v[80:81], v19 offset:26624\n"
      "ds_read_b64 v[82:83], v19 offset:27136\n"
      "ds_read_b64 v[84:85], v19 offset:27648\n"
      "ds_read_b64 v[86:87], v19 offset:28160\n"
      "g2top70_%=:\n"
      "s_waitcnt vmcnt(21) lgkmcnt(0)\n"
      "s_barrier\n"
      "v_add_u32 v16, s55, v9\n"
      "v_add_u32 v17, s55, v10\n"
      "v_add_u32 v19, s55, v11\n"
      "s_add_u32 s55, s55, 28672\n"
      "s_cmp_eq_u32 s55, 143360\n"
      "s_cselect_b32 s55, 0, s55\n"
      "v_mfma_f32_16x16x32_bf16 v[160:163], v[40:43], v[24:27], v[160:163]\n"
      "ds_read_b64_tr_b16 v[88:89], v16 offset:0\n"
      "v_mfma_f32_16x16x32_bf16 v[164:167], v[44:47], v[24:27], v[164:167]\n"
      "ds_read_b64_tr_b16 v[90:91], v16 offset:4096\n"
      "s_add_u32 s58, s54, s36\n"
      "s_add_u32 m0, s58, 0\n"
      "s_add_u32 s38, s50, 0\n"
      "buffer_load_dwordx4 v5, s[40:43], s38 offen sc1 lds\n"
      "v_mfma_f32_16x16x32_bf16 v[168:171], v[48:51], v[24:27], v[168:171]\n"
      "ds_read_b64_tr_b16 v[92:93], v17 offset:0\n"
      "v_mfma_f32_16x16x32_bf16 v[172:175], v[52:55], v[24:27], v[172:175]\n"
      "ds_read_b64_tr_b16 v[94:95], v17 offset:4096\n"
      "v_mfma_f32_16x16x32_bf16 v[176:179], v[56:59], v[24:27], v[176:179]\n"
      "ds_read_b64 v[104:105], v19 offset:16384\n"
      "ds_read_b64 v[106:107], v19 offset:16896\n"
      "s_add_u32 m0, s58, 4096\n"
      "s_add_u32 s38, s50, 131072\n"
      "buffer_load_dwordx4 v5, s[40:43], s38 offen sc1 lds\n"
      "v_mfma_f32_16x16x32_bf16 v[180:183], v[60:63], v[24:27], v[180:183]\n"
      "ds_read_b64 v[108:109], v19 offset:17408\n"
      "ds_read_b64 v[110:111], v19 offset:17920\n"
      "v_mfma_f32_16x16x32_bf16 v[184:187], v[40:43], v[28:31], v[184:187]\n"
      "ds_read_b64 v[112:113], v19 offset:18432\n"
      "ds_read_b64 v[114:115], v19 offset:18944\n"
      "v_mfma_f32_16x16x32_bf16 v[188:191], v[44:47], v[28:31], v[188:191]\n"
      "ds_read_b64 v[116:117], v19 offset:19456\n"
      "ds_read_b64 v[118:119], v19 offset:19968\n"
      "s_add_u32 m0, s58, 8192\n"
      "s_add_u32 s38, s50, 262144\n"
      "buffer_load_dwordx4 v5, s[40:43], s38 offen sc1 lds\n"
      "v_mfma_f32_16x16x32_bf16 v[192:195], v[48:51], v[28:31], v[192:195]\n"
      "ds_read_b64 v[120:121], v19 offset:20480\n"
      "ds_read_b64 v[122:123], v19 offset:20992\n"
      "v_mfma_f32_16x16x32_bf16 v[196:199], v[52:55], v[28:31], v[196:199]\n"
      "ds_read_b64 v[124:125], v19 offset:21504\n"
      "ds_read_b64 v[126:127], v19 offset:22016\n"
      "v_mfma_f32_16x16x32_bf16 v[200:203], v[56:59], v[28:31], v[200:203]\n"
      "ds_read_b64_tr_b16 v[96:97], v16 offset:8192\n"
      "s_add_u32 m0, s58, 12288\n"
      "s_add_u32 s38, s50, 393216\n"
      "buffer_load_dwordx4 v5, s[40:43], s38 offen sc1 lds\n"
      "v_mfma_f32_16x16x32_bf16 v[204:207], v[60:63], v[28:31], v[204:207]\n"
      "ds_read_b64_tr_b16 v[98:99], v16 offset:12288\n"
      "v_mfma_f32_16x16x32_bf16 v[160:163], v[64:67], v[32:35], v[160:163]\n"
      "ds_read_b64_tr_b16 v[100:101], v17 offset:8192\n"
      "v_mfma_f32_16x16x32_bf16 v[164:167], v[68:71], v[32:35], v[164:167]\n"
      "ds_read_b64_tr_b16 v[102:103], v17 offset:12288\n"
      "s_add_u32 m0, s58, 16384\n"
      "s_nop 0\n"
      "buffer_load_dwordx4 v6, s[40:43], s53 offen sc1 lds\n"
      "s_add_u32 s51, s51, 524288\n"
      "s_min_u32 s51, s51, 54001664\n"
      "s_lshr_b32 s52, s51, 8\n"
      "v_mfma_f32_16x16x32_bf16 v[168:171], v[72:75], v[32:35], v[168:171]\n"
      "ds_read_b64 v[128:129], v19 offset:22528\n"
      "ds_read_b64 v[130:131], v19 offset:23040\n"
      "v_mfma_f32_16x16x32_bf16 v[172:175], v[76:79], v[32:35], v[172:175]\n"
      "ds_read_b64 v[132:133], v19 offset:23552\n"
      "ds_read_b64 v[134:135], v19 offset:24064\n"
      "v_mfma_f32_16x16x32_bf16 v[176:179], v[80:83], v[32:35], v[176:179]\n"
      "ds_read_b64 v[136:137], v19 offset:24576\n"
      "ds_read_b64 v[138:139], v19 offset:25088\n"
      "s_add_u32 m0, s58, 20480\n"
      "s_nop 0\n"
      "buffer_load_dwordx4 v7, s[40:43], s53 offen sc1 lds\n"
      "s_xor_b32 s52, s52, s44\n"
      "s_and_b32 s52, s52, 8192\n"
      "s_or_b32 s50, s51, s52\n"
      "v_mfma_f32_16x16x32_bf16 v[180:183], v[84:87], v[32:35], v[180:183]\n"
      "ds_read_b64 v[140:141], v19 offset:25600\n"
      "ds_read_b64 v[142:143], v19 offset:26112\n"
      "v_mfma_f32_16x16x32_bf16 v[184:187], v[64:67], v[36:39], v[184:187]\n"
      "ds_read_b64 v[144:145], v19 offset:26624\n"
      "ds_read_b64 v[146:147], v19 offset:27136\n"
      "v_mfma_f32_16x16x32_bf16 v[188:191], v[68:71], v[36:39], v[188:191]\n"
      "ds_read_b64 v[148:149], v19 offset:27648\n"
      "ds_read_b64 v[150:151], v19 offset:28160\n"
      "s_add_u32 m0, s58, 24576\n"
      "s_nop 0\n"
      "buffer_load_dwordx4 v8, s[40:43], s53 offen sc1 lds\n"
      "s_add_u32 s53, s53, 73728\n"
      "s_add_u32 s54, s54, 28672\n"
      "s_cmp_eq_u32 s54, 143360\n"
      "s_cselect_b32 s54, 0, s54\n"
      "v_mfma_f32_16x16x32_bf16 v[192:195], v[72:75], v[36:39], v[192:195]\n"
      "v_mfma_f32_16x16x32_bf16 v[196:199], v[76:79], v[36:39], v[196:199]\n"
      "v_mfma_f32_16x16x32_bf16 v[200:203], v[80:83], v[36:39], v[200:203]\n"
      "v_mfma_f32_16x16x32_bf16 v[204:207], v[84:87], v[36:39], v[204:207]\n"
      "s_waitcnt vmcnt(21) lgkmcnt(0)\n"
      "s_barrier\n"
      "v_add_u32 v16, s55, v9\n"
      "v_add_u32 v17, s55, v10\n"
      "v_add_u32 v19, s55, v11\n"
      "s_add_u32 s55, s55, 28672\n"
      "s_cmp_eq_u32 s55, 143360\n"
      "s_cselect_b32 s55, 0, s55\n"
      "v_mfma_f32_16x16x32_bf16 v[160:163], v[104:107], v[88:91], v[160:163]\n"
      "ds_read_b64_tr_b16 v[24:25], v16 offset:0\n"
      "v_mfma_f32_16x16x32_bf16 v[164:167], v[108:111], v[88:91], v[164:167]\n"
      "ds_read_b64_tr_b16 v[26:27], v16 offset:4096\n"
      "s_add_u32 s58, s54, s36\n"
      "s_add_u32 m0, s58, 0\n"
      "s_add_u32 s38, s50, 0\n"
      "buffer_load_dwordx4 v5, s[40:43], s38 offen sc1 lds\n"
      "v_mfma_f32_16x16x32_bf16 v[168:171], v[112:115], v[88:91], v[168:171]\n"
      "ds_read_b64_tr_b16 v[28:29], v17 offset:0\n"
      "v_mfma_f32_16x16x32_bf16 v[172:175], v[116:119], v[88:91], v[172:175]\n"
      "ds_read_b64_tr_b16 v[30:31], v17 offset:4096\n"
      "v_mfma_f32_16x16x32_bf16 v[176:179], v[120:123], v[88:91], v[176:179]\n"
      "ds_read_b64 v[40:41], v19 offset:16384\n"
      "ds_read_b64 v[42:43], v19 offset:16896\n"
      "s_add_u32 m0, s58, 4096\n"
      "s_add_u32 s38, s50, 131072\n"
      "buffer_load_dwordx4 v5, s[40:43], s38 offen sc1 lds\n"
      "v_mfma_f32_16x16x32_bf16 v[180:183], v[124:127], v[88:91], v[180:183]\n"
      "ds_read_b64 v[44:45], v19 offset:17408\n"
      "ds_read_b64 v[46:47], v19 offset:17920\n"
      "v_mfma_f32_16x16x32_bf16 v[184:187], v[104:107], v[92:95], v[184:187]\n"
      "ds_read_b64 v[48:49], v19 offset:18432\n"
      "ds_read_b64 v[50:51], v19 offset:18944\n"
      "v_mfma_f32_16x16x32_bf16 v[188:191], v[108:111], v[92:95], v[188:191]\n"
      "ds_read_b64 v[52:53], v19 offset:19456\n"
      "ds_read_b64 v[54:55], v19 offset:19968\n"
      "s_add_u32 m0, s58, 8192\n"
      "s_add_u32 s38, s50, 262144\n"
      "buffer_load_dwordx4 v5, s[40:43], s38 offen sc1 lds\n"
      "v_mfma_f32_16x16x32_bf16 v[192:195], v[112:115], v[92:95], v[192:195]\n"
      "ds_read_b64 v[56:57], v19 offset:20480\n"
      "ds_read_b64 v[58:59], v19 offset:20992\n"
      "v_mfma_f32_16x16x32_bf16 v[196:199], v[116:119], v[92:95], v[196:199]\n"
      "ds_read_b64 v[60:61], v19 offset:21504\n"
      "ds_read_b64 v[62:63], v19 offset:22016\n"
      "v_mfma_f32_16x16x32_bf16 v[200:203], v[120:123], v[92:95], v[200:203]\n"
      "ds_read_b64_tr_b16 v[32:33], v16 offset:8192\n"
      "s_add_u32 m0, s58, 12288\n"
      "s_add_u32 s38, s50, 393216\n"
      "buffer_load_dwordx4 v5, s[40:43], s38 offen sc1 lds\n"
      "v_mfma_f32_16x16x32_bf16 v[204:207], v[124:127], v[92:95], v[204:207]\n"
      "ds_read_b64_tr_b16 v[34:35], v16 offset:12288\n"
      "v_mfma_f32_16x16x32_bf16 v[160:163], v[128:131], v[96:99], v[160:163]\n"
      "ds_read_b64_tr_b16 v[36:37], v17 offset:8192\n"
      "v_mfma_f32_16x16x32_bf16 v[164:167], v[132:135], v[96:99], v[164:167]\n"
      "ds_read_b64_tr_b16 v[38:39], v17 offset:12288\n"
      "s_add_u32 m0, s58, 16384\n"
      "s_nop 0\n"
      "buffer_load_dwordx4 v6, s[40:43], s53 offen sc1 lds\n"
      "s_add_u32 s51, s51, 524288\n"
      "s_min_u32 s51, s51, 54001664\n"
      "s_lshr_b32 s52, s51, 8\n"
      "v_mfma_f32_16x16x32_bf16 v[168:171], v[136:139], v[96:99], v[168:171]\n"
      "ds_read_b64 v[64:65], v19 offset:22528\n"
      "ds_read_b64 v[66:67], v19 offset:23040\n"
      "v_mfma_f32_16x16x32_bf16 v[172:175], v[140:143], v[96:99], v[172:175]\n"
      "ds_read_b64 v[68:69], v19 offset:23552\n"
      "ds_read_b64 v[70:71], v19 offset:24064\n"
      "v_mfma_f32_16x16x32_bf16 v[176:179], v[144:147], v[96:99], v[176:179]\n"
      "ds_read_b64 v[72:73], v19 offset:24576\n"
      "ds_read_b64 v[74:75], v19 offset:25088\n"
      "s_add_u32 m0, s58, 20480\n"
      "s_nop 0\n"
      "buffer_load_dwordx4 v7, s[40:43], s53 offen sc1 lds\n"
      "s_xor_b32 s52, s52, s44\n"
      "s_and_b32 s52, s52, 8192\n"
      "s_or_b32 s50, s51, s52\n"
      "v_mfma_f32_16x16x32_bf16 v[180:183], v[148:151], v[96:99], v[180:183]\n"
      "ds_read_b64 v[76:77], v19 offset:25600\n"
      "ds_read_b64 v[78:79], v19 offset:26112\n"
      "v_mfma_f32_16x16x32_bf16 v[184:187], v[128:131], v[100:103], v[184:187]\n"
      "ds_read_b64 v[80:81], v19 offset:26624\n"
      "ds_read_b64 v[82:83], v19 offset:27136\n"
      "v_mfma_f32_16x16x32_bf16 v[188:191], v[132:135], v[100:103], v[188:191]\n"
      "ds_read_b64 v[84:85], v19 offset:27648\n"
      "ds_read_b64 v[86:87], v19 offset:28160\n"
      "s_add_u32 m0, s58, 24576\n"
      "s_nop 0\n"
      "buffer_load_dwordx4 v8, s[40:43], s53 offen sc1 lds\n"
      "s_add_u32 s53, s53, 73728\n"
      "s_add_u32 s54, s54, 28672\n"
      "s_cmp_eq_u32 s54, 143360\n"
      "s_cselect_b32 s54, 0, s54\n"
      "v_mfma_f32_16x16x32_bf16 v[192:195], v[136:139], v[100:103], v[192:195]\n"
      "v_mfma_f32_16x16x32_bf16 v[196:199], v[140:143], v[100:103], v[196:199]\n"
      "v_mfma_f32_16x16x32_bf16 v[200:203], v[144:147], v[100:103], v[200:203]\n"
      "v_mfma_f32_16x16x32_bf16 v[204:207], v[148:151], v[100:103], v[204:207]\n"
      "s_add_u32 s37, s37, 2\n"
      "s_cmp_lt_u32 s37, 72\n"
      "s_cbranch_scc1 g2top70_%=\n"
      "s_waitcnt vmcnt(0) lgkmcnt(0)\n"
      "s_mov_b32 s44, %[b2lo]\n"
      "s_mov_b32 s45, %[b2hi]\n"
      "s_mov_b32 s46, 4096\n"
      "buffer_load_ushort v24, v12, s[44:47], 0 offen offset:0\n"
      "buffer_load_ushort v25, v12, s[44:47], 0 offen offset:32\n"
      "s_waitcnt vmcnt(0)\n"
      "v_lshlrev_b32 v24, 16, v24\n"
      "v_lshlrev_b32 v25, 16, v25\n"
      "v_add_f32 v160, v160, v24\n"
      "v_add_f32 v161, v161, v24\n"
      "v_add_f32 v162, v162, v24\n"
      "v_add_f32 v163, v163, v24\n"
      "v_cvt_pk_bf16_f32 v28, v160, v161\n"
      "v_cvt_pk_bf16_f32 v29, v162, v163\n"
      "v_add_f32 v164, v164, v24\n"
      "v_add_f32 v165, v165, v24\n"
      "v_add_f32 v166, v166, v24\n"
      "v_add_f32 v167, v167, v24\n"
      "v_cvt_pk_bf16_f32 v30, v164, v165\n"
      "v_cvt_pk_bf16_f32 v31, v166, v167\n"
      "s_mov_b32 s63, 0\n"
      "buffer_store_dwordx2 v[28:29], v13, s[40:43], s63 offen offset:0 sc1\n"
      "buffer_store_dwordx2 v[30:31], v13, s[40:43], s63 offen offset:512 sc1\n"
      "v_add_f32 v168, v168, v24\n"
      "v_add_f32 v169, v169, v24\n"
      "v_add_f32 v170, v170, v24\n"
      "v_add_f32 v171, v171, v24\n"
      "v_cvt_pk_bf16_f32 v32, v168, v169\n"
      "v_cvt_pk_bf16_f32 v33, v170, v171\n"
      "v_add_f32 v172, v172, v24\n"
      "v_add_f32 v173, v173, v24\n"
      "v_add_f32 v174, v174, v24\n"
      "v_add_f32 v175, v175, v24\n"
      "v_cvt_pk_bf16_f32 v34, v172, v173\n"
      "v_cvt_pk_bf16_f32 v35, v174, v175\n"
      "s_mov_b32 s63, 1024\n"
      "buffer_store_dwordx2 v[32:33], v13, s[40:43], s63 offen offset:0 sc1\n"
      "buffer_store_dwordx2 v[34:35], v13, s[40:43], s63 offen offset:512 sc1\n"
      "v_add_f32 v176, v176, v24\n"
      "v_add_f32 v177, v177, v24\n"
      "v_add_f32 v178, v178, v24\n"
      "v_add_f32 v179, v179, v24\n"
      "v_cvt_pk_bf16_f32 v36, v176, v177\n"
      "v_cvt_pk_bf16_f32 v37, v178, v179\n"
      "v_add_f32 v180, v180, v24\n"
      "v_add_f32 v181, v181, v24\n"
      "v_add_f32 v182, v182, v24\n"
      "v_add_f32 v183, v183, v24\n"
      "v_cvt_pk_bf16_f32 v38, v180, v181\n"
      "v_cvt_pk_bf16_f32 v39, v182, v183\n"
      "s_mov_b32 s63, 2048\n"
      "buffer_store_dwordx2 v[36:37], v13, s[40:43], s63 offen offset:0 sc1\n"
      "buffer_store_dwordx2 v[38:39], v13, s[40:43], s63 offen offset:512 sc1\n"
      "v_add_f32 v184, v184, v25\n"
      "v_add_f32 v185, v185, v25\n"
      "v_add_f32 v186, v186, v25\n"
      "v_add_f32 v187, v187, v25\n"
      "v_cvt_pk_bf16_f32 v40, v184, v185\n"
      "v_cvt_pk_bf16_f32 v41, v186, v187\n"
      "v_add_f32 v188, v188, v25\n"
      "v_add_f32 v189, v189, v25\n"
      "v_add_f32 v190, v190, v25\n"
      "v_add_f32 v191, v191, v25\n"
      "v_cvt_pk_bf16_f32 v42, v188, v189\n"
      "v_cvt_pk_bf16_f32 v43, v190, v191\n"
      "s_mov_b32 s63, 18432\n"
      "buffer_store_dwordx2 v[40:41], v13, s[40:43], s63 offen offset:0 sc1\n"
      "buffer_store_dwordx2 v[42:43], v13, s[40:43], s63 offen offset:512 sc1\n"
      "v_add_f32 v192, v192, v25\n"
      "v_add_f32 v193, v193, v25\n"
      "v_add_f32 v194, v194, v25\n"
      "v_add_f32 v195, v195, v25\n"
      "v_cvt_pk_bf16_f32 v44, v192, v193\n"
      "v_cvt_pk_bf16_f32 v45, v194, v195\n"
      "v_add_f32 v196, v196, v25\n"
      "v_add_f32 v197, v197, v25\n"
      "v_add_f32 v198, v198, v25\n"
      "v_add_f32 v199, v199, v25\n"
      "v_cvt_pk_bf16_f32 v46, v196, v197\n"
      "v_cvt_pk_bf16_f32 v47, v198, v199\n"
      "s_mov_b32 s63, 19456\n"
      "buffer_store_dwordx2 v[44:45], v13, s[40:43], s63 offen offset:0 sc1\n"
      "buffer_store_dwordx2 v[46:47], v13, s[40:43], s63 offen offset:512 sc1\n"
      "v_add_f32 v200, v200, v25\n"
      "v_add_f32 v201, v201, v25\n"
      "v_add_f32 v202, v202, v25\n"
      "v_add_f32 v203, v203, v25\n"
      "v_cvt_pk_bf16_f32 v48, v200, v201\n"
      "v_cvt_pk_bf16_f32 v49, v202, v203\n"
      "v_add_f32 v204, v204, v25\n"
      "v_add_f32 v205, v205, v25\n"
      "v_add_f32 v206, v206, v25\n"
      "v_add_f32 v207, v207, v25\n"
      "v_cvt_pk_bf16_f32 v50, v204, v205\n"
      "v_cvt_pk_bf16_f32 v51, v206, v207\n"
      "s_mov_b32 s63, 20480\n"
      "buffer_store_dwordx2 v[48:49], v13, s[40:43], s63 offen offset:0 sc1\n"
      "buffer_store_dwordx2 v[50:51], v13, s[40:43], s63 offen offset:512 sc1\n"
      "s_waitcnt vmcnt(0)\n"
      : "+{v0}"(tid) : [b2lo] "s"(lo32(p.b2)), [b2hi] "s"(hi32(p.b2)) : "memory", "vcc", "scc", CLV4, CLS);
    publish(ws, G2F + b * 128, tid, 65536);
  }
  {
    const int l = tid & 63, w = tid >> 6, c = l & 15, g = l >> 4;
    if (w < 2) wait_ge(ws, G2F + w * 6144, 48, 65536, l, budget);
    __syncthreads();
    v8bf sel;
#pragma unroll
    for (int j = 0; j < 8; ++j) sel[j] = (__bf16)(c == j ? 1.0f : 0.0f);
    const int rowB = 2 * (c & 7) + (g & 1), gqB = 2 * (w & 1) + (c >> 3);
    const unsigned yoA = g < 2 ? g * 512 + (w * 16 + c) * 8 : OOB, yoB = g < 2 ? (w >> 1) * 512 + (gqB * 16 + rowB) * 8 : OOB;
    const unsigned xoA = g < 2 ? (c * 576 + 8 * w + 4 * g) * 2 : OOB, xoB = g < 2 ? (rowB * 576 + 8 * gqB + 4 * (w >> 1)) * 2 : OOB;
    const __amdgpu_buffer_rsrc_t rx = __builtin_amdgcn_make_buffer_rsrc((void*)p.x, 0, 2048 * 1152, 0x00020000);
    const __amdgpu_buffer_rsrc_t ro = __builtin_amdgcn_make_buffer_rsrc(p.out, 0, 2048 * 1152, 0x00020000);
    auto pick = [](int u, unsigned a, unsigned b) { return a ^ ((a ^ b) & (0u - ((0x2010u >> (u % 18)) & 1u))); };
    {
      v2u xv[24], yv[24];
#pragma unroll
      for (int n = 0; n < 24; ++n) {
        const int u = b + 96 * n;
        xv[n] = __builtin_amdgcn_raw_buffer_load_b64(rx, pick(u, xoA, xoB), (u / 18) * 16 * 1152 + 64 * (u % 18), 2);
        yv[n] = __builtin_amdgcn_raw_buffer_load_b64(ws, pick(u, yoA, yoB), YB + u * 1024, 16);
      }
      __builtin_amdgcn_sched_barrier(0);
#pragma unroll
      for (int n = 0; n < 24; ++n) {
        const v4f d = {blo(xv[n].x) + blo(yv[n].x), bhi(xv[n].x) + bhi(yv[n].x), blo(xv[n].y) + blo(yv[n].y), bhi(xv[n].y) + bhi(yv[n].y)};
        *LDSP(v4f, 4096 + n * 4096 + tid * 16) = d;
      }
    }
    int seen = 0;
#pragma unroll 1
    for (int n = 0; n < 24; ++n) {
      const int u = b + 96 * n;
      if (u / 36 >= seen) {
        const bool last = n == 23;
        __syncthreads();
        if (w == 0) {
          int v = wait_ge(ws, QOP + (last ? ((u / 18) & 1) * 4096 : 0), last ? 32 : 64, u / 36 + 1, l, budget, last);
          if (!last) for (int o = 32; o; o >>= 1) v = min(v, __shfl_xor(v, o));
          if (l == 0) *LDSP(int, 64) = max(v, u / 36 + 1);
        }
        __syncthreads();
        seen = __builtin_amdgcn_readfirstlane(*LDSP(int, 64));
      } else {
        __builtin_amdgcn_s_sleep(RPACE);
      }
      v4u t[8];
#pragma unroll
      for (int k = 0; k < 8; ++k) t[k] = __builtin_amdgcn_raw_buffer_load_b128(ws, g * 1024 + w * 256 + c * 16, place(PART + u * 65536) + (k >> 1) * 16384 + (k & 1) * 4096, 16);
      v4f d = *LDSP(v4f, 4096 + n * 4096 + tid * 16);
      __builtin_amdgcn_sched_barrier(0);
#pragma unroll
      for (int k = 0; k < 8; ++k) d = __builtin_amdgcn_mfma_f32_16x16x32_bf16(sel, __builtin_bit_cast(v8bf, t[k]), d, 0, 0, 0);
      const v2u o = {pk2(d.x, d.y), pk2(d.z, d.w)};
      __builtin_amdgcn_raw_buffer_store_b64(o, ro, pick(u, xoA, xoB), (u / 18) * 16 * 1152 + 64 * (u % 18), 0);
    }
  }
}
}

extern "C" void kernel_launch(void* const* d_in, const int* in_sizes, int n_in, void* d_out, int out_size, void* d_ws, size_t ws_size, hipStream_t stream) {
  Params p;
  p.x = (const u16*)d_in[0]; p.mem0 = (const u16*)d_in[1]; p.wq = (const u16*)d_in[2]; p.wk = (const u16*)d_in[3]; p.wv = (const u16*)d_in[4]; p.wo = (const u16*)d_in[5];
  p.w1 = (const u16*)d_in[8]; p.b1 = (const u16*)d_in[9]; p.w2 = (const u16*)d_in[10]; p.b2 = (const u16*)d_in[11];
  p.out = (u16*)d_out;
  p.ws = (char*)(((size_t)d_ws + (2u << 20) - 1) & ~(size_t)((2u << 20) - 1));
  (void)hipFuncSetAttribute((const void*)main_kernel, hipFuncAttributeMaxDynamicSharedMemorySize, LDS_BYTES);
  main_kernel<<<256, 256, LDS_BYTES, stream>>>(p);
}
```
